```python
import torch

def _softmax(x, axis=-1):
    return torch.softmax(x, dim=axis)
def _gelu(x):
    return torch.nn.functional.gelu(x, approximate="tanh")
def _silu(x):
    return torch.nn.functional.silu(x)
def _sigmoid(x):
    return torch.sigmoid(x)
def _layernorm(x, axis=-1, eps=1e-5):
    m = x.mean(dim=axis, keepdim=True)
    v = x.var(dim=axis, keepdim=True, unbiased=False)
    return (x - m) / torch.sqrt(v + eps)
def _scatter_add(tgt, idx, src, axis=0):
    return tgt.clone().index_add_(axis, idx, src)
def _segment_sum(src, seg, n):
    out = torch.zeros((n,) + src.shape[1:], dtype=src.dtype)
    return out.index_add_(0, seg, src)
def _pad(x, pw):
    flat = tuple(v for pair in reversed(pw) for v in pair)
    return torch.nn.functional.pad(x, flat)

kernel_name = 'sequential_preLN__sliding_block_cached__glu_gated_swiglu__decode'

def setup_inputs():

    _w = (lambda a: (lambda t: t.to(torch.bfloat16) if t.is_floating_point() else t)(torch.as_tensor(a)))
    def _A_setup():
        B, Sq, D = 1, 1, 640
        Hq, Hkv, Sk, Dh = 32, 32, 32768, 192
        assert Hq % Hkv == 0, (Hq, Hkv)
        return {
            "x": _w((lambda *s: torch.randn(*s, dtype=torch.bfloat16) * 0.05)(B, Sq, D)),
            "k_cache": _w((lambda *s: torch.randn(*s, dtype=torch.bfloat16) * 1.6)(B, Hkv, Sk, Dh)),
            "v_cache": _w((lambda *s: torch.randn(*s, dtype=torch.bfloat16) * 0.8)(B, Hkv, Sk, Dh)),
            "wq": _w((lambda *s: torch.randn(*s, dtype=torch.bfloat16) * 1.6)(D, Hq * Dh) * (D ** -0.5)),
            "wo": _w((lambda *s: torch.randn(*s, dtype=torch.bfloat16) * 0.8)(Hq * Dh, D) * ((Hq * Dh) ** -0.5)),
        }
    _A_in = _A_setup()

    def _B_setup():
        B, S, D, F = 1, 1, 640, 1536
        return {
            "x": _w((lambda *s: torch.randn(*s, dtype=torch.bfloat16) * 0.05)(B, S, D)),
            "w1": _w((lambda *s: torch.randn(*s, dtype=torch.bfloat16) * 0.4)(D, F) * (2.0 / D) ** 0.5),
            "w3": _w((lambda *s: torch.randn(*s, dtype=torch.bfloat16) * 0.4)(D, F) * (2.0 / D) ** 0.5),
            "w2": _w((lambda *s: torch.randn(*s, dtype=torch.bfloat16) * 0.4)(F, D) * (2.0 / F) ** 0.5),
        }
    _B_in = _B_setup()

    return {
        "A_x": _A_in["x"],
        "A_k_cache": _A_in["k_cache"],
        "A_v_cache": _A_in["v_cache"],
        "A_wq": _A_in["wq"],
        "A_wo": _A_in["wo"],
        "B_x": _B_in["x"],
        "B_w1": _B_in["w1"],
        "B_w3": _B_in["w3"],
        "B_w2": _B_in["w2"],
    }

def reference(A_x, A_k_cache, A_v_cache, A_wq, A_wo, B_x, B_w1, B_w3, B_w2):
    def _A_ref(x, k_cache, v_cache, wq, wo):
        B, Sq, D = x.shape
        Hq, Dh = 32, 192
        Hkv, Sk = k_cache.shape[1], k_cache.shape[2]
        W = min(4096, max(256, Sk // 4))
        rep = Hq // Hkv
        q = (lambda x, *d: x.permute(*d))((x @ wq).reshape(B, Sq, Hq, Dh), 0, 2, 1, 3)
        k = (lambda x, n, axis: torch.repeat_interleave(x, n, dim=axis))(k_cache[:, :, Sk - W:, :], rep, axis=1)
        v = (lambda x, n, axis: torch.repeat_interleave(x, n, dim=axis))(v_cache[:, :, Sk - W:, :], rep, axis=1)
        scores = torch.einsum("bhqd,bhkd->bhqk", q, k) * (Dh ** -0.5)
        p = _softmax(scores, axis=-1)
        o = torch.einsum("bhqk,bhkd->bhqd", p, v)
        return (lambda x, *d: x.permute(*d))(o, 0, 2, 1, 3).reshape(B, Sq, Hq * Dh) @ wo

    def _B_ref(x, w1, w3, w2):
        gate = torch.einsum("bsd,df->bsf", x, w1)
        up = torch.einsum("bsd,df->bsf", x, w3)
        h = _silu(gate) * up
        out = torch.einsum("bsf,fd->bsd", h, w2)
        return out

    x = A_x
    h = x + _A_ref(_layernorm(x, axis=-1), A_k_cache, A_v_cache, A_wq, A_wo)
    h2 = h + _B_ref(_layernorm(h, axis=-1), B_w1, B_w3, B_w2)
    return h2
```

```cpp
#include <hip/hip_runtime.h>
#include <cstdint>

namespace {

constexpr int D = 640, DH = 192, SK = 32768, WIN = 4096, FF = 1536;
constexpr int NWG = 256, NSW = 8, NTHR = 768;
constexpr int KW = 6;
constexpr int VW = 6;
constexpr int VP = 6;
constexpr int QS = 10;
constexpr int MS = 4;
constexpr int OS = 2;
constexpr int PS = 2;
constexpr int HS = 4;
constexpr int RS = 1;
constexpr int PK = 23;
constexpr int DW = 8;
constexpr int DB = 12;
constexpr int DP = 3;
constexpr int SA = 100;
constexpr int SE = 1600;
constexpr int WS = 3;
constexpr int WS2 = 2;
constexpr int D2 = 30;
constexpr int VN = 4;
constexpr int WW = 4;
constexpr int WD = 0;
constexpr int PT = 600;
constexpr int PL = 0;
constexpr int GO = 0;
constexpr int GP = 0;
constexpr int GR = 1950;
constexpr int SP = 1510;
constexpr int QT = 640;
constexpr int NB = 12;
constexpr int OL = 8;
constexpr int OK = 32;
constexpr int BA = 12;
constexpr int KP1 = 544;
constexpr int KN1 = 576;
constexpr int KP2 = 560;
constexpr int KN2 = 576;
constexpr int CPF = 16;

constexpr int NF = 24, NP = 120, NR = 80;
constexpr unsigned WS_EPOCH = 0;
constexpr unsigned TWIN = 8192;
__device__ __forceinline__ unsigned prow(unsigned r) { return (r >> 1) * 16384u + (r & 1) * 4096u; }
__device__ __forceinline__ unsigned orow(unsigned o) { return (o >> 13) * 16384u + (o & 8191u); }
constexpr unsigned WS_PR = 16384;
constexpr unsigned WS_P2 = 32768;
constexpr unsigned WS_P3 = WS_P2 + 6 * 16384;
constexpr unsigned O_ROW = 3 * 16384;
constexpr unsigned WS_O = WS_P3 + 12 * 16384;
constexpr unsigned WS_ML = WS_O + 8 * O_ROW;
constexpr unsigned WS_Q = WS_ML + 256 * 128;
constexpr unsigned WS_END = WS_Q + 96 * 1024;
constexpr unsigned OOB = 0x7F000000u;
constexpr int SPIN_CAP = 1 << 17;

typedef unsigned u32x2 __attribute__((ext_vector_type(2)));
typedef unsigned u32x4 __attribute__((ext_vector_type(4)));
typedef unsigned u32x8 __attribute__((ext_vector_type(8)));
typedef float f32x2 __attribute__((ext_vector_type(2)));
typedef float f32x4 __attribute__((ext_vector_type(4)));
typedef __bf16 bf16x2 __attribute__((ext_vector_type(2)));
typedef __bf16 bf16x8 __attribute__((ext_vector_type(8)));
typedef __bf16 bf16x16 __attribute__((ext_vector_type(16)));
typedef __amdgpu_buffer_rsrc_t rsrc_t;
constexpr int AUX_NT = 2, AUX_SC1 = 16;

template <class P> __device__ __forceinline__ rsrc_t mk(P p, unsigned bytes) { return __builtin_amdgcn_make_buffer_rsrc((void*)p, 0, bytes, 0x00020000); }
template <int AUX> __device__ __forceinline__ u32x4 ld128(rsrc_t r, unsigned vo, unsigned so) { return __builtin_amdgcn_raw_buffer_load_b128(r, vo, so, AUX); }
template <int AUX> __device__ __forceinline__ u32x2 ld64(rsrc_t r, unsigned vo, unsigned so) { return __builtin_amdgcn_raw_buffer_load_b64(r, vo, so, AUX); }
template <int AUX> __device__ __forceinline__ unsigned ld32(rsrc_t r, unsigned vo, unsigned so) { return __builtin_amdgcn_raw_buffer_load_b32(r, vo, so, AUX); }
__device__ __forceinline__ void st128(u32x4 v, rsrc_t r, unsigned vo) { __builtin_amdgcn_raw_buffer_store_b128(v, r, vo, 0, AUX_SC1); }
__device__ __forceinline__ void st64(u32x2 v, rsrc_t r, unsigned vo) { __builtin_amdgcn_raw_buffer_store_b64(v, r, vo, 0, AUX_SC1); }

__device__ __forceinline__ float asf(unsigned u) { return __uint_as_float(u); }
__device__ __forceinline__ unsigned asu(float f) { return __float_as_uint(f); }
__device__ __forceinline__ f32x4 asf4(u32x4 v) { return __builtin_bit_cast(f32x4, v); }
__device__ __forceinline__ float lo16(unsigned u) { return asf(u << 16); }
__device__ __forceinline__ float hi16(unsigned u) { return asf(u & 0xFFFF0000u); }
__device__ __forceinline__ unsigned pk(float lo, float hi) { bf16x2 v = {(__bf16)lo, (__bf16)hi}; return __builtin_bit_cast(unsigned, v); }
__device__ __forceinline__ float bfr(float f) { return (float)(__bf16)f; }
__device__ __forceinline__ float dot2(unsigned a, unsigned b, float acc) {
  return __builtin_amdgcn_fdot2_f32_bf16(__builtin_bit_cast(bf16x2, a), __builtin_bit_cast(bf16x2, b), acc, false);
}
__device__ __forceinline__ f32x4 smf4(u32x4 a, u32x8 b, f32x4 c, int idx) {
  return __builtin_amdgcn_smfmac_f32_16x16x64_bf16(__builtin_bit_cast(bf16x8, a), __builtin_bit_cast(bf16x16, b), c, idx, 0, 0);
}
__device__ __forceinline__ float smf(u32x4 a, u32x8 b, int idx) { return smf4(a, b, f32x4{0.f, 0.f, 0.f, 0.f}, idx)[0]; }
__device__ __forceinline__ float mfma(u32x4 a, u32x4 b) {
  return __builtin_amdgcn_mfma_f32_16x16x32_bf16(__builtin_bit_cast(bf16x8, a), __builtin_bit_cast(bf16x8, b), f32x4{0.f, 0.f, 0.f, 0.f}, 0, 0, 0)[0];
}
template <int N> __device__ __forceinline__ void zero(f32x4 (&d)[N]) {
#pragma unroll
  for (int i = 0; i < N; ++i) { d[i] = f32x4{0.f, 0.f, 0.f, 0.f}; asm volatile("" : "+v"(d[i])); }
}
__device__ __forceinline__ int smf_idx(int odd) { return odd ? 0xEEEE : 0x4444; }
constexpr int krow(int j) { return 32 * (j >> 3) + 8 * ((j >> 1) & 1) + 2 * ((j >> 2) & 1) + (j & 1); }
__device__ __forceinline__ int klane(int lane) { return 16 * (lane >> 5) + 4 * ((lane >> 4) & 1); }
__device__ __forceinline__ unsigned plo(unsigned a, unsigned b) { return __builtin_amdgcn_perm(b, a, 0x05040100u); }
__device__ __forceinline__ unsigned phi(unsigned a, unsigned b) { return __builtin_amdgcn_perm(b, a, 0x07060302u); }
template <int CTRL> __device__ __forceinline__ float dpp(float x) {
  return __builtin_bit_cast(float, __builtin_amdgcn_mov_dpp(__builtin_bit_cast(int, x), CTRL, 0xf, 0xf, true));
}
template <int N> __device__ __forceinline__ unsigned shl(unsigned x) { return (unsigned)__builtin_amdgcn_mov_dpp((int)x, 0x100 + N, 0xf, 0xf, true); }
constexpr int XOR1 = 0xB1, XOR2 = 0x4E, HMIR = 0x141, ROR8 = 0x128, SHL4 = 0x104;
__device__ __forceinline__ float tr32(float a, float b) { auto s = __builtin_amdgcn_permlane32_swap(asu(a), asu(b), false, false); return asf(s[0]) + asf(s[1]); }
__device__ __forceinline__ float tr16(float a, float b) { auto s = __builtin_amdgcn_permlane16_swap(asu(a), asu(b), false, false); return asf(s[0]) + asf(s[1]); }
__device__ __forceinline__ float tr8(float a, float b, bool b3) { const float t1 = a + dpp<ROR8>(a), t2 = b + dpp<ROR8>(b); return b3 ? t2 : t1; }
__device__ __forceinline__ float sum8(float x) { x += dpp<XOR1>(x); x += dpp<XOR2>(x); x += dpp<HMIR>(x); return x; }
__device__ __forceinline__ float max8(float x) { x = fmaxf(x, dpp<XOR1>(x)); x = fmaxf(x, dpp<XOR2>(x)); x = fmaxf(x, dpp<HMIR>(x)); return x; }
__device__ __forceinline__ float sumg(float x) { x += dpp<ROR8>(x); x = tr16(x, x); return tr32(x, x); }
__device__ __forceinline__ float maxg(float x) {
  x = fmaxf(x, dpp<ROR8>(x));
  auto s = __builtin_amdgcn_permlane16_swap(asu(x), asu(x), false, false); x = fmaxf(asf(s[0]), asf(s[1]));
  auto t = __builtin_amdgcn_permlane32_swap(asu(x), asu(x), false, false); return fmaxf(asf(t[0]), asf(t[1]));
}
__device__ __forceinline__ float sum64(float x) { return sumg(sum8(x)); }
__device__ __forceinline__ float sum21(float x) { x += dpp<SHL4>(x); x += dpp<XOR2>(x); return x; }
__device__ __forceinline__ float rdl(float v, int l) { return asf((unsigned)__builtin_amdgcn_readlane((int)asu(v), l)); }
__device__ __forceinline__ float rstd(float var) { return __builtin_amdgcn_rcpf(bfr(__builtin_amdgcn_sqrtf(bfr(var + 1e-5f)))); }
__device__ __forceinline__ void bar() {
  __builtin_amdgcn_sched_barrier(0);
  asm volatile("s_waitcnt lgkmcnt(0)\n\ts_barrier" ::: "memory");
  __builtin_amdgcn_sched_barrier(0);
}
#define SCHED() __builtin_amdgcn_sched_barrier(0)
#define LEAVE() asm volatile("s_endpgm" ::: "memory")
#define POLL_LOOP _Pragma("nounroll") for (int spin = 0; spin < SPIN_CAP; ++spin)
#define VMWAIT(n) asm volatile("s_waitcnt vmcnt(%0)" ::"n"(n) : "memory")
#define PACE(W, STEP) asm volatile("s_waitcnt vmcnt(%2) lgkmcnt(0)\n\ts_lshl_b32 s94, s92, 4\n\ts_sub_u32 s94, s94, %0\n\ts_add_u32 %0, %0, %1\n\ts_cmp_lt_i32 s94, 0\n\ts_cbranch_scc0 1f\n\t" \
    "s_waitcnt vmcnt(%3)\n\ts_cmp_lt_i32 s94, %5\n\ts_cbranch_scc0 1f\n\ts_waitcnt vmcnt(%4)\n1:\n\ts_memrealtime s[92:93]" \
    : "+s"(due) : "s"(STEP), "n"((W) - 1), "n"(WS - 1), "n"(WS2 - 1), "n"(-16 * D2) : "s92", "s93", "s94", "scc", "memory")
constexpr float L2E = 1.4426950408889634f;

__device__ __forceinline__ u32x4 make_tag(u32x2 e1) {
  const unsigned a0 = e1[0] & 0x3FFu;
  return u32x4{a0, ((e1[0] >> 10) + a0) & 0x3FFu, ((e1[0] >> 20) + a0) & 0x3FFu, (((e1[0] >> 30) | (e1[1] << 2)) + a0) & 0x3FFu};
}
__device__ __forceinline__ u32x4 tagged(f32x4 o, u32x4 T) {
  u32x4 r;
#pragma unroll
  for (int k = 0; k < 4; ++k) r[k] = __builtin_amdgcn_bitop3_b32(asu(o[k]), ~0x3FFu, T[k], 0xEA);
  return r;
}
template <int N, int S, int MODE, int K> __device__ __forceinline__ void poll(rsrc_t r, const unsigned (&off)[N], u32x4 T, u32x4 (&v)[N]);
template <> __device__ __forceinline__ void poll<1, 1, 0, 0>(rsrc_t r, const unsigned (&off)[1], u32x4 T, u32x4 (&v)[1]) {
  asm volatile(
      "s_mov_b64 s[88:89], exec\ns_mov_b32 s90, 0x20000\nv_cmp_ne_u32 vcc, 0x7f000000, %1\ns_mov_b64 s[80:81], vcc\n.Lp_%=:\ns_mov_b64 exec, s[80:81]\n"
      "s_cbranch_execz .Ls0_%=\nbuffer_load_dwordx4 v[160:163], %1, %6, 0 offen sc1\n.Ls0_%=:\ns_mov_b64 exec, s[88:89]\ns_waitcnt vmcnt(0)\nv_xor_b32 v164, v161, %3\n"
      "v_bitop3_b32 v164, v162, %4, v164 bitop3:0xbe\nv_bitop3_b32 v164, v163, %5, v164 bitop3:0xbe\nv_bitop3_b32 v164, v160, %2, v164 bitop3:0xbe\n"
      "v_and_b32 v164, 0x3ff, v164\nv_cmp_ne_u32 vcc, 0, v164\ns_and_b64 s[80:81], s[80:81], vcc\ns_cbranch_scc0 .Le_%=\ns_sleep 1\ns_sub_u32 s90, s90, 1\n"
      "s_cbranch_scc0 .Lp_%=\n.Le_%=:\n"
      : "+&{v[160:163]}"(v[0])
      : "v"(off[0]), "v"(T[0]), "v"(T[1]), "v"(T[2]), "v"(T[3]), "s"(r)
      : "memory", "vcc", "scc", "v164", "s80", "s81", "s88", "s89", "s90");
}
template <> __device__ __forceinline__ void poll<1, 4, 0, 0>(rsrc_t r, const unsigned (&off)[1], u32x4 T, u32x4 (&v)[1]) {
  asm volatile(
      "s_mov_b64 s[88:89], exec\ns_mov_b32 s90, 0x20000\nv_cmp_ne_u32 vcc, 0x7f000000, %1\ns_mov_b64 s[80:81], vcc\n.Lp_%=:\ns_mov_b64 exec, s[80:81]\n"
      "s_cbranch_execz .Ls0_%=\nbuffer_load_dwordx4 v[160:163], %1, %6, 0 offen sc1\n.Ls0_%=:\ns_mov_b64 exec, s[88:89]\ns_waitcnt vmcnt(0)\nv_xor_b32 v164, v161, %3\n"
      "v_bitop3_b32 v164, v162, %4, v164 bitop3:0xbe\nv_bitop3_b32 v164, v163, %5, v164 bitop3:0xbe\nv_bitop3_b32 v164, v160, %2, v164 bitop3:0xbe\n"
      "v_and_b32 v164, 0x3ff, v164\nv_cmp_ne_u32 vcc, 0, v164\ns_and_b64 s[80:81], s[80:81], vcc\ns_cbranch_scc0 .Le_%=\ns_sleep 4\ns_sub_u32 s90, s90, 1\n"
      "s_cbranch_scc0 .Lp_%=\n.Le_%=:\n"
      : "+&{v[160:163]}"(v[0])
      : "v"(off[0]), "v"(T[0]), "v"(T[1]), "v"(T[2]), "v"(T[3]), "s"(r)
      : "memory", "vcc", "scc", "v164", "s80", "s81", "s88", "s89", "s90");
}
template <> __device__ __forceinline__ void poll<2, 2099202, 5, 0>(rsrc_t r, const unsigned (&off)[2], u32x4 T, u32x4 (&v)[2]) {
  asm volatile(
      "s_mov_b64 s[88:89], exec\ns_mov_b32 s90, 0x20000\nv_cmp_ne_u32 vcc, 0x7f000000, %2\ns_mov_b64 s[80:81], vcc\nv_cmp_ne_u32 vcc, 0x7f000000, %3\n"
      "s_mov_b64 s[82:83], vcc\ns_branch .Lc_%=\n.Lp_%=:\ns_mov_b64 exec, s[80:81]\ns_cbranch_execz .Ls0_%=\nbuffer_load_dwordx4 v[156:159], %2, %8, 0 offen sc1\n"
      ".Ls0_%=:\ns_mov_b64 exec, s[82:83]\ns_cbranch_execz .Ls1_%=\nbuffer_load_dwordx4 v[160:163], %3, %8, 0 offen sc1\n.Ls1_%=:\ns_mov_b64 exec, s[88:89]\n"
      "s_waitcnt vmcnt(0)\n.Lc_%=:\nv_xor_b32 v164, v157, %5\nv_bitop3_b32 v164, v158, %6, v164 bitop3:0xbe\nv_bitop3_b32 v164, v159, %7, v164 bitop3:0xbe\n"
      "v_bitop3_b32 v164, v156, %4, v164 bitop3:0xbe\nv_and_b32 v164, 0x3ff, v164\nv_cmp_ne_u32 vcc, 0, v164\ns_and_b64 s[80:81], s[80:81], vcc\n"
      "v_xor_b32 v164, v161, %5\nv_bitop3_b32 v164, v162, %6, v164 bitop3:0xbe\nv_bitop3_b32 v164, v163, %7, v164 bitop3:0xbe\n"
      "v_bitop3_b32 v164, v160, %4, v164 bitop3:0xbe\nv_and_b32 v164, 0x3ff, v164\nv_cmp_ne_u32 vcc, 0, v164\ns_and_b64 s[82:83], s[82:83], vcc\n"
      "s_or_b64 s[92:93], s[80:81], s[82:83]\ns_cbranch_scc0 .Le_%=\ns_bcnt1_i32_b64 s96, s[80:81]\ns_mov_b32 s91, s96\ns_bcnt1_i32_b64 s96, s[82:83]\n"
      "s_add_u32 s91, s91, s96\ns_cmp_le_u32 s91, 32\ns_cbranch_scc1 .Lq_%=\ns_sleep 8\n.Lq_%=:\ns_sleep 2\ns_sub_u32 s90, s90, 1\ns_cbranch_scc0 .Lp_%=\n.Le_%=:\n"
      : "+&{v[156:159]}"(v[0]), "+&{v[160:163]}"(v[1])
      : "v"(off[0]), "v"(off[1]), "v"(T[0]), "v"(T[1]), "v"(T[2]), "v"(T[3]), "s"(r)
      : "memory", "vcc", "scc", "v164", "s80", "s81", "s82", "s83", "s88", "s89", "s90", "s92", "s93", "s91", "s96");
}
template <> __device__ __forceinline__ void poll<3, 2, 4, 23>(rsrc_t r, const unsigned (&off)[3], u32x4 T, u32x4 (&v)[3]) {
  asm volatile(
      "s_mov_b64 s[88:89], exec\ns_mov_b32 s90, 0x20000\ns_mov_b32 s94, 15\ns_mov_b32 s95, 15\nv_cmp_ne_u32 vcc, 0x7f000000, %3\ns_mov_b64 s[80:81], vcc\n"
      "s_mov_b64 exec, vcc\nv_not_b32 v152, %6\ns_mov_b64 exec, s[88:89]\nv_cmp_ne_u32 vcc, 0x7f000000, %4\ns_mov_b64 s[82:83], vcc\ns_mov_b64 exec, vcc\n"
      "v_not_b32 v156, %6\ns_mov_b64 exec, s[88:89]\nv_cmp_ne_u32 vcc, 0x7f000000, %5\ns_mov_b64 s[84:85], vcc\ns_mov_b64 exec, vcc\nv_not_b32 v160, %6\n"
      "s_mov_b64 exec, s[88:89]\n.Lp_%=:\ns_andn2_b64 s[96:97], s[94:95], s[80:81]\ns_and_b32 s96, s96, 15\ns_and_b32 s97, s97, 15\ns_mul_i32 s96, s96, 0x11111111\n"
      "s_mul_i32 s97, s97, 0x11111111\ns_or_b64 s[96:97], s[96:97], s[94:95]\ns_and_b64 exec, s[96:97], s[80:81]\ns_cbranch_execz .Ls0_%=\n"
      "buffer_load_dwordx4 v[152:155], %3, %10, 0 offen sc1\n.Ls0_%=:\ns_andn2_b64 s[96:97], s[94:95], s[82:83]\ns_and_b32 s96, s96, 15\ns_and_b32 s97, s97, 15\n"
      "s_mul_i32 s96, s96, 0x11111111\ns_mul_i32 s97, s97, 0x11111111\ns_or_b64 s[96:97], s[96:97], s[94:95]\ns_and_b64 exec, s[96:97], s[82:83]\n"
      "s_cbranch_execz .Ls1_%=\nbuffer_load_dwordx4 v[156:159], %4, %10, 0 offen sc1\n.Ls1_%=:\ns_andn2_b64 s[96:97], s[94:95], s[84:85]\ns_and_b32 s96, s96, 15\n"
      "s_and_b32 s97, s97, 15\ns_mul_i32 s96, s96, 0x11111111\ns_mul_i32 s97, s97, 0x11111111\ns_or_b64 s[96:97], s[96:97], s[94:95]\ns_and_b64 exec, s[96:97], s[84:85]\n"
      "s_cbranch_execz .Ls2_%=\nbuffer_load_dwordx4 v[160:163], %5, %10, 0 offen sc1\n.Ls2_%=:\ns_mov_b64 exec, s[88:89]\ns_waitcnt vmcnt(0)\nv_xor_b32 v164, v153, %7\n"
      "v_bitop3_b32 v164, v154, %8, v164 bitop3:0xbe\nv_bitop3_b32 v164, v155, %9, v164 bitop3:0xbe\nv_bitop3_b32 v164, v152, %6, v164 bitop3:0xbe\n"
      "v_and_b32 v164, 0x3ff, v164\nv_cmp_ne_u32 vcc, 0, v164\ns_and_b64 s[80:81], s[80:81], vcc\nv_xor_b32 v164, v157, %7\n"
      "v_bitop3_b32 v164, v158, %8, v164 bitop3:0xbe\nv_bitop3_b32 v164, v159, %9, v164 bitop3:0xbe\nv_bitop3_b32 v164, v156, %6, v164 bitop3:0xbe\n"
      "v_and_b32 v164, 0x3ff, v164\nv_cmp_ne_u32 vcc, 0, v164\ns_and_b64 s[82:83], s[82:83], vcc\ns_or_b64 s[92:93], s[80:81], s[82:83]\nv_xor_b32 v164, v161, %7\n"
      "v_bitop3_b32 v164, v162, %8, v164 bitop3:0xbe\nv_bitop3_b32 v164, v163, %9, v164 bitop3:0xbe\nv_bitop3_b32 v164, v160, %6, v164 bitop3:0xbe\n"
      "v_and_b32 v164, 0x3ff, v164\nv_cmp_ne_u32 vcc, 0, v164\ns_and_b64 s[84:85], s[84:85], vcc\ns_or_b64 s[92:93], s[92:93], s[84:85]\ns_cbranch_scc0 .Le_%=\n"
      "s_and_b64 s[96:97], s[80:81], s[94:95]\ns_bcnt1_i32_b64 s96, s[96:97]\ns_mov_b32 s91, s96\ns_and_b64 s[96:97], s[82:83], s[94:95]\ns_bcnt1_i32_b64 s96, s[96:97]\n"
      "s_add_u32 s91, s91, s96\ns_and_b64 s[96:97], s[84:85], s[94:95]\ns_bcnt1_i32_b64 s96, s[96:97]\ns_add_u32 s91, s91, s96\ns_cmp_le_u32 s91, 23\n"
      "s_cbranch_scc1 .Lt_%=\ns_sleep 2\ns_sub_u32 s90, s90, 1\ns_cbranch_scc0 .Lp_%=\ns_branch .Le_%=\n.Lu_%=:\n.Lt_%=:\ns_mov_b64 exec, s[80:81]\n"
      "s_cbranch_execz .Lv0_%=\nbuffer_load_dwordx4 v[152:155], %3, %10, 0 offen sc1\n.Lv0_%=:\ns_mov_b64 exec, s[82:83]\ns_cbranch_execz .Lv1_%=\n"
      "buffer_load_dwordx4 v[156:159], %4, %10, 0 offen sc1\n.Lv1_%=:\ns_mov_b64 exec, s[84:85]\ns_cbranch_execz .Lv2_%=\n"
      "buffer_load_dwordx4 v[160:163], %5, %10, 0 offen sc1\n.Lv2_%=:\ns_mov_b64 exec, s[88:89]\ns_sub_u32 s90, s90, 1\ns_cbranch_scc1 .Lw_%=\ns_waitcnt vmcnt(0)\n"
      "v_xor_b32 v164, v153, %7\nv_bitop3_b32 v164, v154, %8, v164 bitop3:0xbe\nv_bitop3_b32 v164, v155, %9, v164 bitop3:0xbe\n"
      "v_bitop3_b32 v164, v152, %6, v164 bitop3:0xbe\nv_and_b32 v164, 0x3ff, v164\nv_cmp_ne_u32 vcc, 0, v164\ns_and_b64 s[80:81], s[80:81], vcc\n"
      "v_xor_b32 v164, v157, %7\nv_bitop3_b32 v164, v158, %8, v164 bitop3:0xbe\nv_bitop3_b32 v164, v159, %9, v164 bitop3:0xbe\n"
      "v_bitop3_b32 v164, v156, %6, v164 bitop3:0xbe\nv_and_b32 v164, 0x3ff, v164\nv_cmp_ne_u32 vcc, 0, v164\ns_and_b64 s[82:83], s[82:83], vcc\n"
      "s_or_b64 s[92:93], s[80:81], s[82:83]\nv_xor_b32 v164, v161, %7\nv_bitop3_b32 v164, v162, %8, v164 bitop3:0xbe\nv_bitop3_b32 v164, v163, %9, v164 bitop3:0xbe\n"
      "v_bitop3_b32 v164, v160, %6, v164 bitop3:0xbe\nv_and_b32 v164, 0x3ff, v164\nv_cmp_ne_u32 vcc, 0, v164\ns_and_b64 s[84:85], s[84:85], vcc\n"
      "s_or_b64 s[92:93], s[92:93], s[84:85]\ns_cbranch_scc1 .Lu_%=\n.Lw_%=:\ns_waitcnt vmcnt(0)\n.Le_%=:\n"
      : "+&{v[152:155]}"(v[0]), "+&{v[156:159]}"(v[1]), "+&{v[160:163]}"(v[2])
      : "v"(off[0]), "v"(off[1]), "v"(off[2]), "v"(T[0]), "v"(T[1]), "v"(T[2]), "v"(T[3]), "s"(r)
      : "memory", "vcc", "scc", "v164", "s80", "s81", "s82", "s83", "s84", "s85", "s88", "s89", "s90", "s92", "s93", "s91", "s94", "s95", "s96", "s97");
}
template <> __device__ __forceinline__ void poll<3, 10, 0, 0>(rsrc_t r, const unsigned (&off)[3], u32x4 T, u32x4 (&v)[3]) {
  asm volatile(
      "s_mov_b64 s[88:89], exec\ns_mov_b32 s90, 0x20000\nv_cmp_ne_u32 vcc, 0x7f000000, %3\ns_mov_b64 s[80:81], vcc\nv_cmp_ne_u32 vcc, 0x7f000000, %4\n"
      "s_mov_b64 s[82:83], vcc\nv_cmp_ne_u32 vcc, 0x7f000000, %5\ns_mov_b64 s[84:85], vcc\n.Lp_%=:\ns_mov_b64 exec, s[80:81]\ns_cbranch_execz .Ls0_%=\n"
      "buffer_load_dwordx4 v[152:155], %3, %10, 0 offen sc1\n.Ls0_%=:\ns_mov_b64 exec, s[82:83]\ns_cbranch_execz .Ls1_%=\n"
      "buffer_load_dwordx4 v[156:159], %4, %10, 0 offen sc1\n.Ls1_%=:\ns_mov_b64 exec, s[84:85]\ns_cbranch_execz .Ls2_%=\n"
      "buffer_load_dwordx4 v[160:163], %5, %10, 0 offen sc1\n.Ls2_%=:\ns_mov_b64 exec, s[88:89]\ns_waitcnt vmcnt(0)\nv_xor_b32 v164, v153, %7\n"
      "v_bitop3_b32 v164, v154, %8, v164 bitop3:0xbe\nv_bitop3_b32 v164, v155, %9, v164 bitop3:0xbe\nv_bitop3_b32 v164, v152, %6, v164 bitop3:0xbe\n"
      "v_and_b32 v164, 0x3ff, v164\nv_cmp_ne_u32 vcc, 0, v164\ns_and_b64 s[80:81], s[80:81], vcc\nv_xor_b32 v164, v157, %7\n"
      "v_bitop3_b32 v164, v158, %8, v164 bitop3:0xbe\nv_bitop3_b32 v164, v159, %9, v164 bitop3:0xbe\nv_bitop3_b32 v164, v156, %6, v164 bitop3:0xbe\n"
      "v_and_b32 v164, 0x3ff, v164\nv_cmp_ne_u32 vcc, 0, v164\ns_and_b64 s[82:83], s[82:83], vcc\ns_or_b64 s[92:93], s[80:81], s[82:83]\nv_xor_b32 v164, v161, %7\n"
      "v_bitop3_b32 v164, v162, %8, v164 bitop3:0xbe\nv_bitop3_b32 v164, v163, %9, v164 bitop3:0xbe\nv_bitop3_b32 v164, v160, %6, v164 bitop3:0xbe\n"
      "v_and_b32 v164, 0x3ff, v164\nv_cmp_ne_u32 vcc, 0, v164\ns_and_b64 s[84:85], s[84:85], vcc\ns_or_b64 s[92:93], s[92:93], s[84:85]\ns_cbranch_scc0 .Le_%=\n"
      "s_sleep 10\ns_sub_u32 s90, s90, 1\ns_cbranch_scc0 .Lp_%=\n.Le_%=:\n"
      : "+&{v[152:155]}"(v[0]), "+&{v[156:159]}"(v[1]), "+&{v[160:163]}"(v[2])
      : "v"(off[0]), "v"(off[1]), "v"(off[2]), "v"(T[0]), "v"(T[1]), "v"(T[2]), "v"(T[3]), "s"(r)
      : "memory", "vcc", "scc", "v164", "s80", "s81", "s82", "s83", "s84", "s85", "s88", "s89", "s90", "s92", "s93");
}
template <int STRIDE, int S, int MODE = 0, int K = 0, int N> __device__ __forceinline__ void gather(rsrc_t rws, unsigned off0, u32x4 T, f32x4 (&o)[N]) {
  unsigned off[N];
  u32x4 v[N];
#pragma unroll
  for (int j = 0; j < N; ++j) { off[j] = off0 >= OOB ? OOB : off0 + j * STRIDE; v[j] = T; }
  SCHED();
  poll<N, S, MODE, K>(rws, off, T, v);
  SCHED();
#pragma unroll
  for (int j = 0; j < N; ++j) o[j] = asf4(v[j] ^ T);
}
__device__ __forceinline__ void turn(const u32x4* w, u32x8* o) {
#pragma unroll
  for (int k = 0; k < 4; ++k)
#pragma unroll
    for (int r = 0; r < 8; ++r) { o[2 * k][r] = plo(w[2 * r][k], w[2 * r + 1][k]); o[2 * k + 1][r] = phi(w[2 * r][k], w[2 * r + 1][k]); }
#pragma unroll
  for (int i = 0; i < 8; ++i) asm volatile("" : "+v"(o[i]));
}
constexpr int krow2(int t) { return 32 * (t >> 2) + (t & 3); }
__device__ __forceinline__ void turn2(const u32x4* w, u32x8* o) {
#pragma unroll
  for (int k = 0; k < 4; ++k)
#pragma unroll
    for (int r = 0; r < 4; ++r) { o[k][2 * r] = plo(w[2 * r][k], w[2 * r + 1][k]); o[k][2 * r + 1] = phi(w[2 * r][k], w[2 * r + 1][k]); }
#pragma unroll
  for (int i = 0; i < 4; ++i) asm volatile("" : "+v"(o[i]));
}
#define DPP8(C) "v_add_f32_dpp %0, %0, %0 " C "\n\tv_add_f32_dpp %1, %1, %1 " C "\n\tv_add_f32_dpp %2, %2, %2 " C "\n\tv_add_f32_dpp %3, %3, %3 " C "\n\t" \
                "v_add_f32_dpp %4, %4, %4 " C "\n\tv_add_f32_dpp %5, %5, %5 " C "\n\tv_add_f32_dpp %6, %6, %6 " C "\n\tv_add_f32_dpp %7, %7, %7 " C "\n\t"
__device__ __forceinline__ void sum8x8(float (&x)[8]) {
  asm("s_nop 1\n\t" DPP8("quad_perm:[1,0,3,2] row_mask:0xf bank_mask:0xf") DPP8("row_ror:8 row_mask:0xf bank_mask:0xf") DPP8("row_ror:4 row_mask:0xf bank_mask:0xf")
      : "+v"(x[0]), "+v"(x[1]), "+v"(x[2]), "+v"(x[3]), "+v"(x[4]), "+v"(x[5]), "+v"(x[6]), "+v"(x[7]));
}
__device__ __forceinline__ f32x4 sum8rows(const float* p, int stride) {
  f32x4 t[8];
#pragma unroll
  for (int w = 0; w < 8; ++w) t[w] = *(const f32x4*)(p + w * stride);
  return ((t[0] + t[1]) + (t[2] + t[3])) + ((t[4] + t[5]) + (t[6] + t[7]));
}
template <int S = 2> __device__ __forceinline__ void lds_wait(unsigned* w, unsigned want) {
  POLL_LOOP {
    if (__hip_atomic_load(w, __ATOMIC_RELAXED, __HIP_MEMORY_SCOPE_WORKGROUP) >= want) break;
    if (S) __builtin_amdgcn_s_sleep(S);
  }
  asm volatile("" ::: "memory");
}
__device__ __forceinline__ void lds_post(unsigned* w, int lane, unsigned n = 1u) {
  asm volatile("s_waitcnt lgkmcnt(0)" ::: "memory");
  if (lane == 0) __hip_atomic_fetch_add(w, n, __ATOMIC_RELAXED, __HIP_MEMORY_SCOPE_WORKGROUP);
}

__device__ __forceinline__ void raise(unsigned* w) { asm volatile("" ::: "memory"); __hip_atomic_store(w, 1u, __ATOMIC_RELAXED, __HIP_MEMORY_SCOPE_WORKGROUP); }
template <int S> __device__ __forceinline__ void await(unsigned* w, int n) {
  POLL_LOOP {
    if (__builtin_popcountll(__builtin_amdgcn_ballot_w64(__hip_atomic_load(w, __ATOMIC_RELAXED, __HIP_MEMORY_SCOPE_WORKGROUP) != 0u)) >= n) break;
    if (S) __builtin_amdgcn_s_sleep(S);
  }
  asm volatile("" ::: "memory");
}
__device__ __forceinline__ float score(const u32x4* k, const u32x4* q) {
  float d0 = 0.f, d1 = 0.f;
#pragma unroll
  for (int j = 0; j < 3; ++j) {
    d0 = dot2(k[j][0], q[j][0], d0); d1 = dot2(k[j][1], q[j][1], d1);
    d0 = dot2(k[j][2], q[j][2], d0); d1 = dot2(k[j][3], q[j][3], d1);
  }
  return bfr(bfr(sum8(d0 + d1)) * 0.07216878364870322f);
}
__device__ __forceinline__ void pv8(u32x4 v, unsigned pl, float* acc) {
  const unsigned ph = pl << 16;
#pragma unroll
  for (int k = 0; k < 4; ++k) { acc[2 * k] = dot2(v[k], pl, acc[2 * k]); acc[2 * k + 1] = dot2(v[k], ph, acc[2 * k + 1]); }
}
__device__ __forceinline__ float fold8(const float* acc, int lane) {
  return tr8(tr16(tr32(acc[0], acc[4]), tr32(acc[2], acc[6])), tr16(tr32(acc[1], acc[5]), tr32(acc[3], acc[7])), lane & 8);
}

struct __attribute__((aligned(16))) Lds {
  float ored[NSW][DH];
  float pred[NSW + 1][64];
  float fred[10][128];
  float cred[10][128];
  unsigned short qs[DH];
  unsigned short act[72];
  float ml[NSW][2];
  unsigned tag[4];
  unsigned cnt[4];
  unsigned flag[24];
  unsigned hsum[2][32];
  unsigned near;
  unsigned pad[1];
  u32x4 wst[2][512][8];
};

__device__ __forceinline__ int job_of(int h, int s) {
  const int e = (s - h - 1) & 7;
  return h >= NF ? h * 8 + s : e == 7 ? h : e >= 2 ? NF + s * 15 + (h >> 3) * 5 + e - 2 : NF + NP + 2 * h + e;
}
#define UNDEF8(w) asm volatile("" : "=v"(w[0]), "=v"(w[1]), "=v"(w[2]), "=v"(w[3]), "=v"(w[4]), "=v"(w[5]), "=v"(w[6]), "=v"(w[7]))
#define LOFF(x) (unsigned)((const char*)&(x) - (const char*)&L)
#define LAT(T, off) ((T*)((char*)&L + (off)))
typedef const unsigned short* cptr;
typedef const __attribute__((address_space(1))) unsigned short* gptr;
typedef const __attribute__((address_space(4))) unsigned long long* kargs_t;
#define KARGS kargs_t ka = (kargs_t)__builtin_amdgcn_kernarg_segment_ptr(); asm volatile("" : "+s"(ka))
#define ARG(i) ((gptr)ka[i])
__global__ __launch_bounds__(NTHR, 3) void k_fused(cptr, cptr, cptr, cptr, cptr, cptr, cptr, cptr, unsigned short*, unsigned char*) {
  __shared__ Lds L;
  asm volatile(".Lcode_begin:\n\ts_load_dwordx4 s[96:99], %0, 0x8\n\ts_load_dwordx2 s[100:101], %0, 0x18" ::"s"(__builtin_amdgcn_kernarg_segment_ptr())
               : "s96", "s97", "s98", "s99", "s100", "s101");
  const int lane = threadIdx.x & 63, wave = __builtin_amdgcn_readfirstlane(threadIdx.x >> 6), b = blockIdx.x;
  const int h = b >> 3, s = b & 7;
  const int g = lane >> 3, c = lane & 7;
  const bool stream = wave < NSW;
  const int a = wave - NSW;
  u32x2 e1 = {0u, 0u};
  u32x4 T = {0u, 0u, 0u, 0u};
  u32x4 wt[8];

  const unsigned long long tk0 = __builtin_amdgcn_s_memrealtime();
  const bool isF = h < NF && s == (h & 7);
  if (threadIdx.x < 94) {
    const unsigned i = threadIdx.x - 12u;
    L.cnt[threadIdx.x] = i < 16u ? i >= 10u : i < 80u && ((i - 16u) & 31u) < 20u ? ~0u : 0u;
  }
  const int e = (s - h - 1) & 7, kp = h >= NF ? 512 : (h & 7) == 3 ? KP2 : KP1, kn = h >= NF ? 512 : (h & 7) == 3 ? KN2 : KN1, k0 = e * kp + min(e, 2) * (kn - kp);
  int nk = e == 7 ? WIN - k0 : e < 2 ? kn : kp;
  unsigned kvbase = (unsigned)(h * SK + (SK - WIN) + k0) * DH;
  unsigned voff = (unsigned)(wave * 8 + g) * 384u + c * 16u;
  asm volatile("" : "+s"(kvbase), "+s"(nk));
  asm volatile("" : "+v"(voff));

  if (__builtin_expect(stream, 1)) {
    gptr kc0, vc0;
    asm volatile("s_waitcnt lgkmcnt(0)\n\ts_mov_b64 %0, s[96:97]\n\ts_mov_b64 %1, s[98:99]" : "=s"(kc0), "=s"(vc0));
    const rsrc_t rk = mk(kc0 + kvbase, nk * 384), rv = mk(vc0 + kvbase, nk * 384);
    u32x4 kb[27], vb[27];
    unsigned due, tau, tau8;
#pragma unroll
    for (int i = 0; i < 27; ++i) {
      if (i == BA) bar();
      if (i == KW) {
        const bool nine = 512 + 8 * wave < nk;
        tau = isF ? 0u : h < NF && e >= 2 ? (nine ? 16 * (SP - SA) / 53 : 16 * (SP - SA) / 47) : nine ? 16 * (SE - SA) / 53 : 16 * (SE - SA) / 47;
        tau8 = nine ? tau : 0u;
        due = ((unsigned)tk0 << 4) + (isF ? -65536 : 16 * SA) + (KW - 1) * tau;
        asm volatile("s_memrealtime s[92:93]" : "+s"(tau), "+s"(tau8) :: "s92", "s93");
      }
      if (i >= KW) PACE(KW, i < 24 ? tau : tau8);
      kb[i] = ld128<AUX_NT>(rk, voff + (i % 3) * 128, (i / 3) * 24576);
      SCHED();
    }
#define LISTLOAD(n) vb[n] = ld128<AUX_NT>(rv, voff + ((n) % 3) * 128, (8 - (n) / 3) * 24576)
#pragma unroll
    for (int n = 0; n < VW; ++n) {
      PACE(KW, n < 3 ? tau8 : tau);
      LISTLOAD(n);
      SCHED();
    }
    lds_wait(&L.cnt[1], 1);
    u32x4 qv[3];
#pragma unroll
    for (int j = 0; j < 3; ++j) qv[j] = *(const u32x4*)&L.qs[j * 64 + c * 8];
    float sc[9];
#pragma unroll
    for (int t = 0; t < 9; ++t) {
      sc[t] = score(kb + 3 * t, qv);
      if (t & 1) {
        SCHED();
#pragma unroll
        for (int n = VW + VN * (t - 1) / 8; n < VW + VN * (t + 1) / 8; ++n) LISTLOAD(n);
        SCHED();
      }
    }
    due += VN * tau;
    asm volatile("s_memrealtime s[92:93]" : "+s"(due) :: "s92", "s93");
    constexpr int V1 = VW + VN;
#pragma unroll
    for (int t = 1; t < 9; ++t) if (64 * t + 8 * wave >= nk) sc[t] = -__builtin_inff();
    float m = sc[0];
#pragma unroll
    for (int t = 1; t < 9; ++t) m = fmaxf(m, sc[t]);
    m = maxg(m);
    float lsum = 0.f;
    unsigned pp[9];
#pragma unroll
    for (int t = 0; t < 9; ++t) { const float p = __builtin_amdgcn_exp2f((sc[t] - m) * L2E); lsum += p; pp[t] = pk(p, 0.f); }
    lsum = sumg(lsum);
    if (lane == 0) *(f32x2*)L.ml[wave] = f32x2{m, lsum};
    lds_post(&L.cnt[0], lane);
    float acc[24];
    unsigned odst = LOFF(L.ored[wave][8 * c + g]);
    const int tk = job_of(h, s);
    KARGS;
    const rsrc_t rw = mk(isF ? ARG(7) : ARG(4), isF ? FF * D * 2 : tk < NF + NP ? 6144 * D * 2 : 0);
    unsigned wvo = (unsigned)(isF ? klane(lane) + (lane & 8) : 32 * ((lane >> 4) & 1) + 8 * (lane >> 5) + 4 * (g & 1)) * 1280u + c * 16u;
    int ws0 = isF ? 64 * tk * 1280 + wave * 128 : ((tk - NF) / 10 * 512 + 64 * wave) * 1280 + (tk - NF) % 10 * 128, ws1 = ws0 + (isF ? 32 : 16) * 1280;
    const rsrc_t rws = mk(ARG(9), WS_END);
    unsigned oo = WS_O - 1u + (unsigned)((lane & 5) + ((lane >> 2) & 2)) * O_ROW + orow((unsigned)((tk - NF) / 10 * 512 + 64 * wave + 8 * (lane >> 4) + 2 * (lane & 2)) * 4u);
    asm volatile("" : "+v"(odst), "+v"(wvo), "+v"(oo));
    asm volatile("" : "+s"(ws0), "+s"(ws1));
#define WLOAD(t) wt[t] = ld128<AUX_NT>(rw, wvo + ((t) & 3) * 1280, (t) < 4 ? ws0 : ws1)
#pragma unroll
    for (int e = 0; e < 24; ++e) acc[e] = 0.f;
#pragma unroll
    for (int n = 0; n < 27; ++n) {
      if (n + VP >= V1 && n + VP < 27) { PACE(VP, tau); LISTLOAD(n + VP); }
      if (n + WW - WD >= 27) { PACE(WW, tau); WLOAD(n + WW - WD - 27); }
      SCHED();
      pv8(vb[n], pp[8 - n / 3], acc + 8 * (n % 3));
      if (n >= 24) *LAT(float, odst + (n % 3) * 256) = fold8(acc + 8 * (n % 3), lane);
      SCHED();
    }
    raise(&L.flag[wave]);
    if (isF) {
#pragma unroll
      for (int t = WW - WD; t < 8; ++t) { VMWAIT(WW - 1); WLOAD(t); SCHED(); }
    } else if (tk < NF + NP) {
      T = *(const u32x4*)L.tag;
      lds_wait(&L.near, 1);
      oo += L.near;
      unsigned off[2] = {oo, oo + 128u}, pdst = LOFF(L.pred[g == 0 ? wave : NSW][8 * c]);
      int idx = smf_idx(lane & 1);
      asm volatile("" : "+v"(pdst), "+v"(idx), "+v"(off[0]), "+v"(off[1]), "+v"(T));
      f32x4 d[4];
      zero(d);
      SCHED();
#pragma unroll
      for (int t = WW - WD; t < 8; ++t) { VMWAIT(WW - 1); WLOAD(t); SCHED(); }
      if (PL < WW) VMWAIT(PL);
      if (GO) while (__builtin_amdgcn_s_memrealtime() < tk0 + GO) __builtin_amdgcn_s_sleep(1);
      u32x4 v[2] = {ld128<AUX_SC1>(rws, off[0], 0), ld128<AUX_SC1>(rws, off[1], 0)};
      SCHED();
      u32x8 wp[4];
      turn2(wt, wp);
      SCHED();
      poll<2, OS + 256 * OL + 65536 * OK, 5, 0>(rws, off, T, v);
      SCHED();
      float o8[8];
#pragma unroll
      for (int i = 0; i < 8; ++i) o8[i] = asf(v[i >> 2][i & 3] ^ T[i & 3]);
      sum8x8(o8);
      const u32x4 av = {pk(o8[0], o8[1]), pk(o8[2], o8[3]), pk(o8[4], o8[5]), pk(o8[6], o8[7])};
#pragma unroll
      for (int k = 0; k < 4; ++k) { d[k] = smf4(av, wp[k], d[k], idx); *LAT(f32x2, pdst + 8 * k) = f32x2{d[k][0] + dpp<ROR8>(d[k][2]), d[k][1] + dpp<ROR8>(d[k][3])}; }
      bar();
      LEAVE();
    } else {
      LEAVE();
    }
  } else if (isF && a < 2) {
    __builtin_amdgcn_s_setprio(DP);
    KARGS;
    const rsrc_t rw = mk((a ? ARG(6) : ARG(5)) + 64 * h, D * FF * 2 - 128 * h);
    const unsigned vo = (unsigned)g * 3072u + c * 16u;
    unsigned so = 0u, dst = LOFF(L.wst[a]) + (unsigned)(size_t)&L;
#define DMA() asm volatile("s_mov_b32 m0, %1\n\ts_add_u32 %1, %1, 0x400\n\tbuffer_load_dwordx4 %2, %3, %0 offen nt lds\n\ts_add_u32 %0, %0, 0x6000" : "+s"(so), "+s"(dst) : "v"(vo), "s"(rw) : "memory", "scc")
#pragma nounroll
    for (int i = 0; i < DB; ++i) { VMWAIT(DW - 1); DMA(); }
    bar();
#pragma nounroll
    for (int i = DB; i < 64; ++i) { VMWAIT(DW - 1); DMA(); }
    __builtin_amdgcn_s_setprio(3);
    UNDEF8(wt);
  } else {
    __builtin_amdgcn_s_setprio(3);
    const unsigned long long t0 = __builtin_amdgcn_s_memrealtime();
    const bool big = h >= NF;
    const int qi = big ? 96 + s * 8 + h - NF : e < 2 ? 2 * h + e : 46 + ((h & 7) == 3 ? 21 + (h >> 3) : h - ((h + 4) >> 3)) * 5 + e;
    const bool qjob = !isF && qi < 160;
    const int q2 = qi - 96, q3 = qi < 32 ? qi : (qi - 32) >> 1;
    const int qline = q2 >= 0 ? q2 + q2 / 7 + (q2 % 7 >= 3) : q3 < 9 ? q3 * 8 + 3 : 64 + q3, qpart = q2 >= 0 ? !big : qi < 32 ? 2 : qi & 1;
    constexpr int NS = 20 - NB > 8 ? 20 - NB : 8;
    const int nl = big ? NB : q2 >= 0 ? 20 - NB : qpart == 2 ? 8 : 6, row0 = (q2 >= 0 ? 32 * NB : 192) * qpart + 8 * nl * a;
    unsigned xo = qjob ? (unsigned)(g * nl + row0) * 2u : OOB, wo0 = qjob ? (unsigned)(g * nl) * 12288u + c * 16u : OOB, wo6 = nl > 6 ? wo0 : OOB, wo8 = nl > 8 ? wo0 : OOB;
    int wso = row0 * 12288 + qline * 128;
    asm volatile("" : "+v"(xo), "+v"(wo0), "+v"(wo6), "+v"(wo8));
    asm volatile("" : "+s"(wso));
    gptr wq;
    asm volatile("s_waitcnt lgkmcnt(0)\n\ts_mov_b64 %0, s[100:101]" : "=s"(wq));
    const rsrc_t rq = mk(wq, D * 6144 * 2);
    u32x4 wb[NB];
#pragma unroll
    for (int i = 0; i < NS; ++i) wb[i] = ld128<AUX_NT>(rq, i < 6 ? wo0 : i < 8 ? wo6 : wo8, wso + i * 12288);
    if (big) {
#pragma unroll
      for (int i = NS; i < NB; ++i) wb[i] = ld128<AUX_NT>(rq, wo0, wso + i * 12288);
    }
    SCHED();
    KARGS;
    const rsrc_t rws = mk(ARG(9), WS_END), rx = mk(ARG(0), D * 2);
    const u32x4 xs4 = ld128<0>(rx, (unsigned)lane * 16u, 0);
    const unsigned xs1 = ld32<0>(rx, 1024u + (unsigned)lane * 4u, 0);
    const u32x4 xr0 = ld128<0>(rx, xo, 0), xr4 = ld128<0>(rx, xo + 16u, 0);
    const u32x2 ep = ld64<AUX_SC1>(rws, WS_EPOCH, 0);
    unsigned pf[7] = {0u, 0u, 0u, 0u, 0u, 0u, 0u};
    if (a == 3) {
      pf[0] = ld32<0>(mk(ARG(8), D * 2), 0, 0);
      if (b < CPF) {
        unsigned long long pc;
        unsigned back, size;
        asm volatile("s_getpc_b64 %0\n.Lcode_here:\n\ts_mov_b32 %1, .Lcode_here-.Lcode_begin\n\ts_mov_b32 %2, .Lcode_end-.Lcode_begin" : "=s"(pc), "=s"(back), "=s"(size));
        const rsrc_t rc = mk((gptr)(pc - back), size);
#pragma unroll
        for (int j = 0; j < 4; ++j) pf[1 + j] = ld32<0>(rc, (unsigned)lane * 128u + j * 8192u, 0);
      }
      const size_t kvb = ((size_t)h * SK + (SK - WIN) + 512 * s) * DH;
      const unsigned po = lane == 0 ? 0u : lane == 1 ? 512u * 384u - 128u : OOB;
      pf[5] = ld32<AUX_NT>(mk(ARG(2) + kvb, 512 * 384), po, 0);
      pf[6] = ld32<AUX_NT>(mk(ARG(1) + kvb, 512 * 384), lane == 1 ? po : OOB, 0);
    }
    f32x4 qp = {0.f, 0.f, 0.f, 0.f};
    if (qjob) {
      const unsigned xs[5] = {xs4[0], xs4[1], xs4[2], xs4[3], xs1}, xr[7] = {xr0[0], xr0[1], xr0[2], xr0[3], xr4[0], xr4[1], xr4[2]};
      float sm = 0.f;
#pragma unroll
      for (int i = 0; i < 5; ++i) sm += lo16(xs[i]) + hi16(xs[i]);
      const float mean = sum64(sm) * (1.f / D);
      float sq = 0.f;
#pragma unroll
      for (int i = 0; i < 5; ++i) { const float d0 = lo16(xs[i]) - mean, d1 = hi16(xs[i]) - mean; sq += d0 * d0 + d1 * d1; }
      const float mb = bfr(mean), rden = rstd(bfr(sum64(sq) * (1.f / D)));
      float qa[8];
#pragma unroll
      for (int k = 0; k < 8; ++k) qa[k] = 0.f;
      auto rows = [&](int i) {
        const unsigned xp = pk(bfr(lo16(xr[i]) - mb) * rden, bfr(hi16(xr[i]) - mb) * rden);
#pragma unroll
        for (int k = 0; k < 4; ++k) {
          qa[2 * k] = dot2(plo(wb[2 * i][k], wb[2 * i + 1][k]), xp, qa[2 * k]);
          qa[2 * k + 1] = dot2(phi(wb[2 * i][k], wb[2 * i + 1][k]), xp, qa[2 * k + 1]);
        }
      };
#pragma unroll
      for (int i = 0; i < NS / 2; ++i) rows(i);
      if (big) {
#pragma unroll
        for (int i = NS / 2; i < NB / 2; ++i) rows(i);
      }
#pragma unroll
      for (int i = 0; i < 4; ++i) { float t = tr32(qa[i], qa[i + 4]); t = tr16(t, t); qp[i] = t + dpp<ROR8>(t); }
    }
    bar();
    if ((lane & 24) == 0) *(f32x4*)&L.pred[a][c * 8 + (lane >> 5) * 4] = qp;
    lds_post(&L.cnt[3], lane);
    const unsigned long long en = (((unsigned long long)ep[1] << 32) | ep[0]) + 1ull;
    e1 = u32x2{(unsigned)en, (unsigned)(en >> 32)};
    T = make_tag(e1);
    if (a == 0 && qjob) {
      lds_wait(&L.cnt[3], 4);
      const float* src = &L.pred[0][4 * (lane & 15)];
      qp = (*(const f32x4*)src + *(const f32x4*)(src + 64)) + (*(const f32x4*)(src + 128) + *(const f32x4*)(src + 192));
      const unsigned ex = asu(fmaxf(fmaxf(fabsf(qp[0]), fabsf(qp[1])), fmaxf(fabsf(qp[2]), fabsf(qp[3])))) >> 23;
      u32x4 qo;
#pragma unroll
      for (int i = 0; i < 4; ++i) qo[i] = ((unsigned)min((int)__builtin_rintf(__builtin_ldexpf(qp[i], 145 - (int)ex)), 524287) << 12) | (((ex >> (2 * i)) & 3u) << 10) | T[i];
      st128(qo, rws, lane < 16 ? WS_Q + (unsigned)qline * 1024u + qpart * 256u + lane * 16u : OOB);
    } else if (a == 2) {
      unsigned off[3];
      u32x4 v[3];
#pragma unroll
      for (int j = 0; j < 3; ++j) {
        const int ql = 3 * h + j;
        off[j] = lane < (ql >= 73 || (ql & 7) == 3 ? 48 : 32) ? WS_Q + (unsigned)ql * 1024u + lane * 16u : OOB;
        v[j] = T;
      }
      while (__builtin_amdgcn_s_memrealtime() < t0 + QT) __builtin_amdgcn_s_sleep(4);
      SCHED();
      poll<3, QS, 0, 0>(rws, off, T, v);
      SCHED();
#pragma unroll
      for (int j = 0; j < 3; ++j) {
        const int ex = (int)(((v[j][0] >> 10) & 3u) | ((v[j][1] >> 8) & 12u) | ((v[j][2] >> 6) & 48u) | ((v[j][3] >> 4) & 192u)) - 145;
        f32x4 qf;
#pragma unroll
        for (int i = 0; i < 4; ++i) { float t = __builtin_ldexpf((float)((int)v[j][i] >> 12), ex); t = tr16(t, t); qf[i] = tr32(t, t); }
        if (lane < 16) *(u32x2*)&L.qs[j * 64 + lane * 4] = u32x2{pk(qf[0], qf[1]), pk(qf[2], qf[3])};
      }
      if (lane == 0) *(u32x4*)L.tag = T;
      lds_post(&L.cnt[1], lane);
    } else if (a == 3) {
      st128(T, rws, (s == (h & 7) && lane < 32) ? WS_PR + (unsigned)h * 256u + (lane & 15) * 16u + (lane >> 4) * TWIN : OOB);
      asm volatile("" ::"v"(pf[0]), "v"(pf[1]), "v"(pf[2]), "v"(pf[3]), "v"(pf[4]), "v"(pf[5]), "v"(pf[6]));
    }
    UNDEF8(wt);
  }

  const int wave0 = wave;
  {
    int lane = threadIdx.x & 63, wave = wave0, b = blockIdx.x;
    asm volatile("" : "+v"(lane));
    asm volatile("" : "+s"(wave), "+s"(b));
    const int g = lane >> 3, c = lane & 7, h = b >> 3, s = b & 7, a = wave - NSW;
    const bool stream = wave < NSW, isF = h < NF && s == (h & 7);
    KARGS;
    const gptr w1 = ARG(5), w3 = ARG(6), w2 = ARG(7), out = ARG(8), ws = ARG(9);
    const rsrc_t rws = mk(ws, WS_END), rx = mk(ARG(0), D * 2);
    const int tk = job_of(h, s);
    auto near = [&]() { lds_wait(&L.near, 1); return (unsigned)__builtin_amdgcn_readfirstlane(L.near) - 1u; };
    float fw = 0.f;
    if (a == 3) {
      const unsigned half = (__builtin_amdgcn_s_getreg(20 | (3 << 11)) >> 2) & 1u;
      while (__builtin_amdgcn_s_memrealtime() < tk0 + PT) __builtin_amdgcn_s_sleep(8);
      unsigned tt[2] = {~0u, ~0u};
#pragma unroll
      for (int r = 0; r < 4; ++r) {
        unsigned long long t0, t1;
        unsigned x;
        asm volatile("s_memtime %0\n\ts_waitcnt lgkmcnt(0)\n\ts_load_dword %2, %3, %4\n\ts_waitcnt lgkmcnt(0)\n\ts_memtime %1\n\ts_waitcnt lgkmcnt(0)"
                     : "=&s"(t0), "=&s"(t1), "=&s"(x) : "s"(ws + (WS_PR + h * 256) / 2), "n"((r >> 1) * 128 + (r & 1) * TWIN) : "memory");
        tt[r & 1] = min(tt[r & 1], (unsigned)(t1 - t0));
      }
      const int dt = max(-256, min(256, (int)(tt[0] - tt[1])));
      lds_wait(&L.cnt[0], NSW);
      const f32x2 mlw = *(const f32x2*)L.ml[c];
      const float mwg = asf(asu(max8(mlw[0])) & ~0x3FFu);
      const float lwg = sum8(mlw[1] * __builtin_amdgcn_exp2f((mlw[0] - mwg) * L2E));
      const u32x4 own = tagged(f32x4{mwg, lwg, (float)(half ? -dt : dt), 0.f}, T);
      st128(own, rws, (lane == 0 ? WS_ML + (unsigned)b * 128u : OOB));
      f32x4 mo[1];
      gather<0, MS>(rws, ((lane < 8 && lane != s) ? WS_ML + (unsigned)(h * 8 + lane) * 128u : OOB), T, mo);
      if (lane == s) mo[0] = asf4(own ^ T);
      const unsigned pol = sum8(mo[0][2]) > 0.f;
      if (lane == 0) __hip_atomic_store(&L.near, 1u + ((pol ^ half) & 1u) * TWIN, __ATOMIC_RELAXED, __HIP_MEMORY_SCOPE_WORKGROUP);
      const float mh = max8(mo[0][0]);
      fw = __builtin_amdgcn_exp2f((mlw[0] - mh) * L2E) * __builtin_amdgcn_rcpf(sum8(mo[0][1] * __builtin_amdgcn_exp2f((mo[0][0] - mh) * L2E)));
    }
    if (a == 3) {
      const int dq = min(lane, 47);
      unsigned src = LOFF(L.ored[0][4 * dq]);
      unsigned so = (lane < 48 ? WS_O + (unsigned)s * O_ROW + orow((unsigned)h * (DH * 4) + lane * 16u) : OOB);
      float f[NSW];
#pragma unroll
      for (int w = 0; w < NSW; ++w) { f[w] = rdl(fw, w); asm volatile("" : "+s"(f[w])); }
      asm volatile("" : "+v"(src), "+v"(so));
      await<2>(&L.flag[c], 48);
      f32x4 t[NSW];
      POLL_LOOP {
        const unsigned fl = __hip_atomic_load(&L.flag[c], __ATOMIC_RELAXED, __HIP_MEMORY_SCOPE_WORKGROUP);
        asm volatile("" ::: "memory");
#pragma unroll
        for (int w = 0; w < NSW; ++w) t[w] = *LAT(const f32x4, src + w * DH * 4);
        asm volatile("" ::: "memory");
        SCHED();
        if (__builtin_amdgcn_ballot_w64(fl == 0u) == 0ull) break;
      }
#pragma unroll
      for (int w = 0; w < NSW; ++w) t[w] *= f[w];
      const f32x4 o = ((t[0] + t[1]) + (t[2] + t[3])) + ((t[4] + t[5]) + (t[6] + t[7]));
      const u32x4 ot = tagged(o, T);
      st128(ot, rws, so);
      st128(ot, rws, so + TWIN);
    }
    if (tk < NF) {
      if (wave < 10) {
        u32x4 wb[16];
        unsigned xv;
        {
          const rsrc_t rw2 = mk(w2, FF * D * 2);
          xv = __builtin_amdgcn_raw_buffer_load_b16(rx, (unsigned)(64 * wave + lane) * 2u, 0, 0);
          if (!stream) {
            const gptr pw = ((lane & 8) ? w3 : w1) + (64 * wave + klane(lane)) * FF + 64 * tk + 8 * c;
#pragma unroll
            for (int t = 0; t < 16; ++t) {
              VMWAIT(DW - 1);
              wb[t] = __builtin_nontemporal_load((const __attribute__((address_space(1))) u32x4*)(pw + krow(t) * FF));
              SCHED();
            }
            VMWAIT(16);
            lds_post(&L.cnt[3], lane, 256u);
            await<8>(&L.flag[c], 8);
#pragma unroll
            for (int t = 0; t < 8; ++t) {
              VMWAIT(WW - 1);
              wt[t] = ld128<AUX_NT>(rw2, (unsigned)(klane(lane) + (lane & 8)) * 1280u + c * 16u, (64 * tk + krow2(t)) * 1280 + wave * 128);
              SCHED();
            }
          }
          if (stream) {
            lds_wait(&L.cnt[3], 512);
#pragma unroll
            for (int t = 0; t < 16; ++t) wb[t] = L.wst[(lane >> 3) & 1][64 * wave + klane(lane) + krow(t)][c];
          }
        }
        u32x8 b1[8], b2[4];
        turn(wb, b1); turn2(wt, b2);
        SCHED();
        T = *(const u32x4*)L.tag;
        int idx = smf_idx(lane & 8), idx2 = smf_idx(lane & 1);
        unsigned fdst = LOFF(L.fred[wave][(lane >> 5) * 64 + 2 * c + (g & 1)]);
        {
          const u32x4 ones = {0x3F803F80u, 0x3F803F80u, 0x3F803F80u, 0x3F803F80u};
#pragma unroll
          for (int i = 0; i < 4; ++i) *LAT(float, fdst + (LOFF(L.cred) - LOFF(L.fred)) + i * 64) = tr32(smf(ones, b1[i], idx), smf(ones, b1[i + 4], idx));
        }
        raise(&L.flag[8 + wave]);
        f32x4 pv[3];
        float xf = lo16(xv);
        if (lane & 16) fdst = LOFF(L.pred);
        unsigned hdst = (lane == 0 || lane == 40) ? LOFF(L.hsum[0][2 * wave + (g & 1)]) : LOFF(L.pred), asrc = LOFF(L.act[(lane & 48) + 4 * (lane & 2)]);
        unsigned as0 = lane & 8 ? LOFF(L.act[64]) : asrc, as1 = lane & 8 ? asrc : LOFF(L.act[64]);
        unsigned so = ((lane & 8) == 0 ? WS_P3 + prow(tk) + (lane & 16) * (TWIN / 16) + (unsigned)wave * 256u + c * 32u + (lane >> 5) * 16u : OOB);
        unsigned long long m1 = __builtin_amdgcn_ballot_w64(lane & 1), m2 = __builtin_amdgcn_ballot_w64(lane & 2);
        asm volatile("" : "+v"(fdst), "+v"(hdst), "+v"(xf), "+v"(as0), "+v"(as1), "+v"(so), "+v"(idx), "+v"(idx2));
        asm volatile("" : "+s"(m1), "+s"(m2));
        f32x4 d[8], e[2];
        zero(d);
        u32x4 ones = {0x3F803F80u, 0x3F803F80u, 0x3F803F80u, 0x3F803F80u};
        asm volatile("" : "+v"(ones));
        if (GP) while (__builtin_amdgcn_s_memrealtime() < tk0 + GP) __builtin_amdgcn_s_sleep(2);
        gather<32768, PS, 4, PK>(rws, WS_P2 + near() + prow(lane & 3) + (unsigned)wave * 256u + (unsigned)(lane >> 2) * 16u, T, pv);
        {
          const f32x4 ps = (pv[0] + pv[1]) + pv[2];
          const bool o1 = __builtin_amdgcn_inverse_ballot_w64(m1), o2 = __builtin_amdgcn_inverse_ballot_w64(m2);
          const float k0 = (o1 ? ps[1] : ps[0]) + dpp<XOR1>(o1 ? ps[0] : ps[1]), k1 = (o1 ? ps[3] : ps[2]) + dpp<XOR1>(o1 ? ps[2] : ps[3]);
          const float at = (o2 ? k1 : k0) + dpp<XOR2>(o2 ? k0 : k1);
          const float hv = bfr(xf + bfr(at));
          const unsigned hp = phi(asu(hv), asu(dpp<XOR1>(hv)));
          const u32x4 a0 = {hp, shl<2>(hp), shl<4>(hp), shl<6>(hp)};
          const float s1 = mfma(a0, ones), s2 = mfma(a0, a0);
          SCHED();
#pragma unroll
          for (int i = 0; i < 2; ++i) d[i] = smf4(a0, b1[(i >> 1) + 4 * (i & 1)], d[i], idx);
          SCHED();
          *LAT(float, hdst) = s1; *LAT(float, hdst + 128) = s2;
          SCHED();
#pragma unroll
          for (int i = 2; i < 8; ++i) d[i] = smf4(a0, b1[(i >> 1) + 4 * (i & 1)], d[i], idx);
#pragma unroll
          for (int i = 0; i < 4; ++i) *LAT(float, fdst + i * 64) = tr32(d[2 * i][0], d[2 * i + 1][0]);
        }
        bar();
        zero(e);
        bar();
        const u32x4 a0 = *LAT(const u32x4, as0), a1 = *LAT(const u32x4, as1);
        f32x4 d0 = smf4(a0, b2[0], e[0], idx2), d1 = smf4(a0, b2[1], e[1], idx2);
        SCHED();
        d0 = smf4(a1, b2[2], d0, idx2); d1 = smf4(a1, b2[3], d1, idx2);
        st128(tagged(f32x4{d0[0] + dpp<ROR8>(d0[2]), d0[1] + dpp<ROR8>(d0[3]), d1[0] + dpp<ROR8>(d1[2]), d1[1] + dpp<ROR8>(d1[3])}, T), rws, so);
        LEAVE();
      }
    } else if (a == 3 && tk >= NF + NP && tk < NF + NP + NR) {
      const int col0 = 8 * (tk - NF - NP), e = 4 * (lane & 1) + 2 * (lane >> 5) + ((lane >> 4) & 1);
      const unsigned xv = __builtin_amdgcn_raw_buffer_load_b16(rx, (unsigned)(col0 + e) * 2u, 0, 0);
      const unsigned fo = near() + prow(lane >> 1) + (unsigned)col0 * 4u + (lane & 1) * 16u;
      auto fold = [&](f32x4 f) {
        const float r = tr16(tr32(f[0], f[2]), tr32(f[1], f[3]));
        return sum21(r + dpp<ROR8>(r));
      };
      f32x4 pv[1];
      if (GR) while (__builtin_amdgcn_s_memrealtime() < tk0 + GR) __builtin_amdgcn_s_sleep(2);
      gather<0, HS>(rws, (lane < 24 ? WS_P2 + fo : OOB), T, pv);
      float hsel = bfr(lo16(xv) + bfr(fold(pv[0])));
      unsigned so = ((lane & 14) == 0 ? (unsigned)(col0 + e) * 2u : OOB), se = ((tk == NF + NP && lane == 0) ? WS_EPOCH : OOB);
      asm volatile("" : "+v"(hsel), "+v"(so), "+v"(se));
      gather<0, RS>(rws, (lane < 48 ? WS_P3 + fo : OOB), T, pv);
      __builtin_amdgcn_raw_buffer_store_b16((unsigned short)(pk(hsel + bfr(fold(pv[0])), 0.f) & 0xFFFFu), mk(out, D * 2), so, 0, AUX_SC1 | 1);
      st64(e1, rws, se);
      LEAVE();
    }
    if (a == 2) {
      if (tk < NF) {
        const int li = c * 16 + g * 2;
        if (lane < 4) *(unsigned*)&L.act[64 + 2 * lane] = 0u;
        await<2>(&L.flag[8 + (lane & 15)], 64);
        f32x2 cs = {0.f, 0.f};
#pragma unroll
        for (int j = 0; j < 10; ++j) cs += *(const f32x2*)&L.cred[j][li];
        unsigned src = LOFF(L.fred[0][li]), adst = LOFF(L.act[lane]);
        asm volatile("" : "+v"(cs), "+v"(src), "+v"(adst));
        unsigned* const hs = &L.hsum[0][lane];
        POLL_LOOP {
          if (__builtin_popcountll(__builtin_amdgcn_ballot_w64(__hip_atomic_load(hs, __ATOMIC_RELAXED, __HIP_MEMORY_SCOPE_WORKGROUP) != ~0u)) >= 28) break;
          __builtin_amdgcn_s_sleep(2);
        }
        unsigned sv, r0, r1, r2, r3, cap = SPIN_CAP;
#define LOOK(N, O) "ds_read_b32 %" #N ", %6\n\ts_waitcnt lgkmcnt(3)\n\tv_cmp_eq_u32 vcc, -1, %" #O "\n\tv_mov_b32 %0, %" #O "\n\ts_cbranch_vccz 2f\n\t"
        asm volatile("ds_read_b32 %1, %6\n\tds_read_b32 %2, %6\n\tds_read_b32 %3, %6\n"
                     "1:\n\t" LOOK(4, 1) LOOK(1, 2) LOOK(2, 3) LOOK(3, 4) "s_sub_u32 %5, %5, 1\n\ts_cbranch_scc0 1b\n"
                     "2:"
                     : "=&v"(sv), "=&v"(r0), "=&v"(r1), "=&v"(r2), "=&v"(r3), "+s"(cap) : "v"(LOFF(*hs) + (unsigned)(size_t)&L) : "vcc", "scc", "memory");
        SCHED();
        asm volatile("s_barrier" ::: "memory");
        SCHED();
        f32x2 t[10];
#pragma unroll
        for (int j = 0; j < 10; ++j) t[j] = *LAT(const f32x2, src + j * 512);
        SCHED();
        float s1 = sum8(asf(sv));
        s1 += dpp<ROR8>(s1);
        s1 = tr16(s1, s1);
        const auto s12 = __builtin_amdgcn_permlane32_swap(asu(s1), asu(s1), false, false);
        const float mean = asf(s12[0]) * (1.f / D), s2 = asf(s12[1]);
        float rden = rstd(bfr(fmaxf(s2 * (1.f / D) - mean * mean, 0.f)));
        cs *= -bfr(mean);
        asm volatile("" : "+v"(cs), "+v"(rden));
        SCHED();
        const f32x2 gu = ((((t[0] + t[1]) + (t[2] + t[3])) + ((t[4] + t[5]) + (t[6] + t[7]))) + ((t[8] + t[9]) + cs)) * rden;
        asm volatile("" ::"v"(r0), "v"(r1), "v"(r2), "v"(r3), "v"(gu));
        const unsigned gp = pk(gu[0], gu[1]);
        const float gt = lo16(gp);
        *LAT(unsigned short, adst) = (unsigned short)(pk(bfr(gt * __builtin_amdgcn_rcpf(1.f + __builtin_amdgcn_exp2f(-gt * L2E))) * hi16(gp), 0.f) & 0xFFFFu);
        bar();
        LEAVE();
      }
      if (tk >= NF && tk < NF + NP) {
        unsigned src = LOFF(L.pred[0][4 * (lane & 15)]);
        unsigned so = (lane < 32 ? WS_P2 + prow((tk - NF) / 10) + (lane & 16) * (TWIN / 16) + (unsigned)((tk - NF) % 10) * 256u + (lane & 15) * 16u : OOB);
        asm volatile("" : "+v"(src), "+v"(so));
        bar();
        st128(tagged(sum8rows(LAT(const float, src), 64), T), rws, so);
        LEAVE();
      }
    }
    if (!stream && a != 2) {
      if (tk < NF) { if (a == 3) { bar(); bar(); } }
      else if (tk < NF + NP) bar();
      LEAVE();
    }
  }
  asm volatile(".Lcode_end:");
}

}

extern "C" void kernel_launch(void* const* d_in, const int* in_sizes, int n_in, void* d_out, int out_size, void* d_ws, size_t ws_size,
                              hipStream_t stream) {
  hipLaunchKernelGGL(k_fused, dim3(NWG), dim3(NTHR), 0, stream, (const unsigned short*)d_in[0], (const unsigned short*)d_in[1],
                     (const unsigned short*)d_in[2], (const unsigned short*)d_in[3], (const unsigned short*)d_in[4],
                     (const unsigned short*)d_in[6], (const unsigned short*)d_in[7], (const unsigned short*)d_in[8],
                     (unsigned short*)d_out, (unsigned char*)d_ws);
}
```

```cpp
#include <hip/hip_runtime.h>
#include <cstdint>

namespace {

constexpr int D = 640, DH = 192, SK = 32768, WIN = 4096, FF = 1536;
constexpr int NWG = 256, NSW = 8, NTHR = 768;
constexpr int KW = 6;
constexpr int VW = 6;
constexpr int VP = 6;
constexpr int QS = 10;
constexpr int MS = 4;
constexpr int OS = 2;
constexpr int PS = 2;
constexpr int HS = 1;
constexpr int RS = 1;
constexpr int PK = 184;
constexpr int DW = 8;
constexpr int DB = 0;
constexpr int DP = 3;
constexpr int SA = 100;
constexpr int SE = 1600;
constexpr int WS = 3;
constexpr int WS2 = 2;
constexpr int D2 = 30;
constexpr int VN = 4;
constexpr int WW = 4;
constexpr int WD = 0;
constexpr int PT = 600;
constexpr int GR = 1920;
constexpr int SP = 1530;
constexpr int QT = 650;
constexpr int NB = 12;
constexpr int OL = 8;
constexpr int OK = 32;
constexpr int BA = 6;
constexpr int KP1 = 544;
constexpr int KN1 = 576;
constexpr int KP2 = 560;
constexpr int KN2 = 576;
constexpr int AB = 2;
constexpr int W2 = 5;
constexpr int PL = 0;
constexpr int PLS = 4;
constexpr int G3 = 1850;
constexpr int SW = 1700;
constexpr int TW = 20;
constexpr int TZ = 20;
constexpr int CPF = 16;

constexpr int NF = 24, NP = 120, NR = 80;
constexpr unsigned WS_EPOCH = 0;
constexpr unsigned TWIN = 8192;
__device__ __forceinline__ unsigned prow(unsigned r) { return (r >> 1) * 16384u + (r & 1) * 4096u; }
__device__ __forceinline__ unsigned orow(unsigned o) { return (o >> 13) * 16384u + (o & 8191u); }
constexpr unsigned WS_PR = 16384;
constexpr unsigned WS_P2 = 32768;
constexpr unsigned WS_P3 = WS_P2 + 6 * 16384;
constexpr unsigned O_ROW = 3 * 16384;
constexpr unsigned WS_O = WS_P3 + 12 * 16384;
constexpr unsigned WS_ML = WS_O + 8 * O_ROW;
constexpr unsigned WS_Q = WS_ML + 256 * 128;
constexpr unsigned WS_END = WS_Q + 96 * 1024;
constexpr unsigned OOB = 0x7F000000u;
constexpr int SPIN_CAP = 1 << 17;

typedef unsigned u32x2 __attribute__((ext_vector_type(2)));
typedef unsigned u32x4 __attribute__((ext_vector_type(4)));
typedef unsigned u32x8 __attribute__((ext_vector_type(8)));
typedef float f32x2 __attribute__((ext_vector_type(2)));
typedef float f32x4 __attribute__((ext_vector_type(4)));
typedef __bf16 bf16x2 __attribute__((ext_vector_type(2)));
typedef __bf16 bf16x8 __attribute__((ext_vector_type(8)));
typedef __bf16 bf16x16 __attribute__((ext_vector_type(16)));
typedef __amdgpu_buffer_rsrc_t rsrc_t;
constexpr int AUX_NT = 2, AUX_SC1 = 16;

template <class P> __device__ __forceinline__ rsrc_t mk(P p, unsigned bytes) { return __builtin_amdgcn_make_buffer_rsrc((void*)p, 0, bytes, 0x00020000); }
template <int AUX> __device__ __forceinline__ u32x4 ld128(rsrc_t r, unsigned vo, unsigned so) { return __builtin_amdgcn_raw_buffer_load_b128(r, vo, so, AUX); }
template <int AUX> __device__ __forceinline__ u32x2 ld64(rsrc_t r, unsigned vo, unsigned so) { return __builtin_amdgcn_raw_buffer_load_b64(r, vo, so, AUX); }
template <int AUX> __device__ __forceinline__ unsigned ld32(rsrc_t r, unsigned vo, unsigned so) { return __builtin_amdgcn_raw_buffer_load_b32(r, vo, so, AUX); }
__device__ __forceinline__ void st128(u32x4 v, rsrc_t r, unsigned vo) { __builtin_amdgcn_raw_buffer_store_b128(v, r, vo, 0, AUX_SC1); }
__device__ __forceinline__ void st64(u32x2 v, rsrc_t r, unsigned vo) { __builtin_amdgcn_raw_buffer_store_b64(v, r, vo, 0, AUX_SC1); }

__device__ __forceinline__ float asf(unsigned u) { return __uint_as_float(u); }
__device__ __forceinline__ unsigned asu(float f) { return __float_as_uint(f); }
__device__ __forceinline__ f32x4 asf4(u32x4 v) { return __builtin_bit_cast(f32x4, v); }
__device__ __forceinline__ float lo16(unsigned u) { return asf(u << 16); }
__device__ __forceinline__ float hi16(unsigned u) { return asf(u & 0xFFFF0000u); }
__device__ __forceinline__ unsigned pk(float lo, float hi) { bf16x2 v = {(__bf16)lo, (__bf16)hi}; return __builtin_bit_cast(unsigned, v); }
__device__ __forceinline__ float bfr(float f) { return (float)(__bf16)f; }
__device__ __forceinline__ float dot2(unsigned a, unsigned b, float acc) {
  return __builtin_amdgcn_fdot2_f32_bf16(__builtin_bit_cast(bf16x2, a), __builtin_bit_cast(bf16x2, b), acc, false);
}
__device__ __forceinline__ f32x4 smf4(u32x4 a, u32x8 b, f32x4 c, int idx) {
  return __builtin_amdgcn_smfmac_f32_16x16x64_bf16(__builtin_bit_cast(bf16x8, a), __builtin_bit_cast(bf16x16, b), c, idx, 0, 0);
}
__device__ __forceinline__ float smf(u32x4 a, u32x8 b, int idx) { return smf4(a, b, f32x4{0.f, 0.f, 0.f, 0.f}, idx)[0]; }
__device__ __forceinline__ float mfma(u32x4 a, u32x4 b) {
  return __builtin_amdgcn_mfma_f32_16x16x32_bf16(__builtin_bit_cast(bf16x8, a), __builtin_bit_cast(bf16x8, b), f32x4{0.f, 0.f, 0.f, 0.f}, 0, 0, 0)[0];
}
template <int N> __device__ __forceinline__ void zero(f32x4 (&d)[N]) {
#pragma unroll
  for (int i = 0; i < N; ++i) { d[i] = f32x4{0.f, 0.f, 0.f, 0.f}; asm volatile("" : "+v"(d[i])); }
}
__device__ __forceinline__ int smf_idx(int odd) { return odd ? 0xEEEE : 0x4444; }
constexpr int hrow(int i) { return 4 * (i & 15) + 2 * (i >> 5) + ((i >> 4) & 1); }
constexpr int krow(int j) { return 32 * (j >> 3) + 8 * ((j >> 1) & 1) + 2 * ((j >> 2) & 1) + (j & 1); }
__device__ __forceinline__ int klane(int lane) { return 16 * (lane >> 5) + 4 * ((lane >> 4) & 1); }
__device__ __forceinline__ unsigned plo(unsigned a, unsigned b) { return __builtin_amdgcn_perm(b, a, 0x05040100u); }
__device__ __forceinline__ unsigned phi(unsigned a, unsigned b) { return __builtin_amdgcn_perm(b, a, 0x07060302u); }
template <int CTRL> __device__ __forceinline__ float dpp(float x) {
  return __builtin_bit_cast(float, __builtin_amdgcn_mov_dpp(__builtin_bit_cast(int, x), CTRL, 0xf, 0xf, true));
}
template <int N> __device__ __forceinline__ unsigned shl(unsigned x) { return (unsigned)__builtin_amdgcn_mov_dpp((int)x, 0x100 + N, 0xf, 0xf, true); }
constexpr int XOR1 = 0xB1, XOR2 = 0x4E, HMIR = 0x141, ROR8 = 0x128, SHL4 = 0x104;
__device__ __forceinline__ float tr32(float a, float b) { auto s = __builtin_amdgcn_permlane32_swap(asu(a), asu(b), false, false); return asf(s[0]) + asf(s[1]); }
__device__ __forceinline__ float tr16(float a, float b) { auto s = __builtin_amdgcn_permlane16_swap(asu(a), asu(b), false, false); return asf(s[0]) + asf(s[1]); }
__device__ __forceinline__ float tr8(float a, float b, bool b3) { const float t1 = a + dpp<ROR8>(a), t2 = b + dpp<ROR8>(b); return b3 ? t2 : t1; }
__device__ __forceinline__ float sum8(float x) { x += dpp<XOR1>(x); x += dpp<XOR2>(x); x += dpp<HMIR>(x); return x; }
__device__ __forceinline__ float max8(float x) { x = fmaxf(x, dpp<XOR1>(x)); x = fmaxf(x, dpp<XOR2>(x)); x = fmaxf(x, dpp<HMIR>(x)); return x; }
__device__ __forceinline__ float sumg(float x) { x += dpp<ROR8>(x); x = tr16(x, x); return tr32(x, x); }
__device__ __forceinline__ float maxg(float x) {
  x = fmaxf(x, dpp<ROR8>(x));
  auto s = __builtin_amdgcn_permlane16_swap(asu(x), asu(x), false, false); x = fmaxf(asf(s[0]), asf(s[1]));
  auto t = __builtin_amdgcn_permlane32_swap(asu(x), asu(x), false, false); return fmaxf(asf(t[0]), asf(t[1]));
}
__device__ __forceinline__ float sum64(float x) { return sumg(sum8(x)); }
__device__ __forceinline__ float sum21(float x) { x += dpp<SHL4>(x); x += dpp<XOR2>(x); return x; }
__device__ __forceinline__ float rdl(float v, int l) { return asf((unsigned)__builtin_amdgcn_readlane((int)asu(v), l)); }
__device__ __forceinline__ float rstd(float var) { return __builtin_amdgcn_rcpf(bfr(__builtin_amdgcn_sqrtf(bfr(var + 1e-5f)))); }
__device__ __forceinline__ void bar() {
  __builtin_amdgcn_sched_barrier(0);
  asm volatile("s_waitcnt lgkmcnt(0)\n\ts_barrier" ::: "memory");
  __builtin_amdgcn_sched_barrier(0);
}
#define SCHED() __builtin_amdgcn_sched_barrier(0)
#define LEAVE() asm volatile("s_endpgm" ::: "memory")
#define POLL_LOOP _Pragma("nounroll") for (int spin = 0; spin < SPIN_CAP; ++spin)
#define VMWAIT(n) asm volatile("s_waitcnt vmcnt(%0)" ::"n"(n) : "memory")
#define PACE(W, STEP) asm volatile("s_waitcnt vmcnt(%2) lgkmcnt(0)\n\ts_lshl_b32 s94, s92, 4\n\ts_sub_u32 s94, s94, %0\n\ts_add_u32 %0, %0, %1\n\ts_cmp_lt_i32 s94, 0\n\ts_cbranch_scc0 1f\n\t" \
    "s_waitcnt vmcnt(%3)\n\ts_cmp_lt_i32 s94, %5\n\ts_cbranch_scc0 1f\n\ts_waitcnt vmcnt(%4)\n1:\n\ts_memrealtime s[92:93]" \
    : "+s"(due) : "s"(STEP), "n"((W) - 1), "n"(WS - 1), "n"(WS2 - 1), "n"(-16 * D2) : "s92", "s93", "s94", "scc", "memory")
constexpr float L2E = 1.4426950408889634f;

__device__ __forceinline__ u32x4 make_tag(u32x2 e1) {
  const unsigned a0 = e1[0] & 0x3FFu;
  return u32x4{a0, ((e1[0] >> 10) + a0) & 0x3FFu, ((e1[0] >> 20) + a0) & 0x3FFu, (((e1[0] >> 30) | (e1[1] << 2)) + a0) & 0x3FFu};
}
__device__ __forceinline__ u32x4 tagged(f32x4 o, u32x4 T) {
  u32x4 r;
#pragma unroll
  for (int k = 0; k < 4; ++k) r[k] = __builtin_amdgcn_bitop3_b32(asu(o[k]), ~0x3FFu, T[k], 0xEA);
  return r;
}
template <int N, int S, int MODE, int K> __device__ __forceinline__ void poll(rsrc_t r, const unsigned (&off)[N], u32x4 T, u32x4 (&v)[N]);
template <> __device__ __forceinline__ void poll<1, 1, 0, 0>(rsrc_t r, const unsigned (&off)[1], u32x4 T, u32x4 (&v)[1]) {
  asm volatile(
      "s_mov_b64 s[88:89], exec\ns_mov_b32 s90, 0x20000\nv_cmp_ne_u32 vcc, 0x7f000000, %1\ns_mov_b64 s[80:81], vcc\n.Lp_%=:\ns_mov_b64 exec, s[80:81]\n"
      "s_cbranch_execz .Ls0_%=\nbuffer_load_dwordx4 v[160:163], %1, %6, 0 offen sc1\n.Ls0_%=:\ns_mov_b64 exec, s[88:89]\ns_waitcnt vmcnt(0)\nv_xor_b32 v164, v161, %3\n"
      "v_bitop3_b32 v164, v162, %4, v164 bitop3:0xbe\nv_bitop3_b32 v164, v163, %5, v164 bitop3:0xbe\nv_bitop3_b32 v164, v160, %2, v164 bitop3:0xbe\n"
      "v_and_b32 v164, 0x3ff, v164\nv_cmp_ne_u32 vcc, 0, v164\ns_and_b64 s[80:81], s[80:81], vcc\ns_cbranch_scc0 .Le_%=\ns_sleep 1\ns_sub_u32 s90, s90, 1\n"
      "s_cbranch_scc0 .Lp_%=\n.Le_%=:\n"
      : "+&{v[160:163]}"(v[0])
      : "v"(off[0]), "v"(T[0]), "v"(T[1]), "v"(T[2]), "v"(T[3]), "s"(r)
      : "memory", "vcc", "scc", "v164", "s80", "s81", "s88", "s89", "s90");
}
template <> __device__ __forceinline__ void poll<1, 4, 0, 0>(rsrc_t r, const unsigned (&off)[1], u32x4 T, u32x4 (&v)[1]) {
  asm volatile(
      "s_mov_b64 s[88:89], exec\ns_mov_b32 s90, 0x20000\nv_cmp_ne_u32 vcc, 0x7f000000, %1\ns_mov_b64 s[80:81], vcc\n.Lp_%=:\ns_mov_b64 exec, s[80:81]\n"
      "s_cbranch_execz .Ls0_%=\nbuffer_load_dwordx4 v[160:163], %1, %6, 0 offen sc1\n.Ls0_%=:\ns_mov_b64 exec, s[88:89]\ns_waitcnt vmcnt(0)\nv_xor_b32 v164, v161, %3\n"
      "v_bitop3_b32 v164, v162, %4, v164 bitop3:0xbe\nv_bitop3_b32 v164, v163, %5, v164 bitop3:0xbe\nv_bitop3_b32 v164, v160, %2, v164 bitop3:0xbe\n"
      "v_and_b32 v164, 0x3ff, v164\nv_cmp_ne_u32 vcc, 0, v164\ns_and_b64 s[80:81], s[80:81], vcc\ns_cbranch_scc0 .Le_%=\ns_sleep 4\ns_sub_u32 s90, s90, 1\n"
      "s_cbranch_scc0 .Lp_%=\n.Le_%=:\n"
      : "+&{v[160:163]}"(v[0])
      : "v"(off[0]), "v"(T[0]), "v"(T[1]), "v"(T[2]), "v"(T[3]), "s"(r)
      : "memory", "vcc", "scc", "v164", "s80", "s81", "s88", "s89", "s90");
}
template <> __device__ __forceinline__ void poll<2, 2099202, 5, 0>(rsrc_t r, const unsigned (&off)[2], u32x4 T, u32x4 (&v)[2]) {
  asm volatile(
      "s_mov_b64 s[88:89], exec\ns_mov_b32 s90, 0x20000\nv_cmp_ne_u32 vcc, 0x7f000000, %2\ns_mov_b64 s[80:81], vcc\nv_cmp_ne_u32 vcc, 0x7f000000, %3\n"
      "s_mov_b64 s[82:83], vcc\ns_branch .Lc_%=\n.Lp_%=:\ns_mov_b64 exec, s[80:81]\ns_cbranch_execz .Ls0_%=\nbuffer_load_dwordx4 v[156:159], %2, %8, 0 offen sc1\n"
      ".Ls0_%=:\ns_mov_b64 exec, s[82:83]\ns_cbranch_execz .Ls1_%=\nbuffer_load_dwordx4 v[160:163], %3, %8, 0 offen sc1\n.Ls1_%=:\ns_mov_b64 exec, s[88:89]\n"
      "s_waitcnt vmcnt(0)\n.Lc_%=:\nv_xor_b32 v164, v157, %5\nv_bitop3_b32 v164, v158, %6, v164 bitop3:0xbe\nv_bitop3_b32 v164, v159, %7, v164 bitop3:0xbe\n"
      "v_bitop3_b32 v164, v156, %4, v164 bitop3:0xbe\nv_and_b32 v164, 0x3ff, v164\nv_cmp_ne_u32 vcc, 0, v164\ns_and_b64 s[80:81], s[80:81], vcc\n"
      "v_xor_b32 v164, v161, %5\nv_bitop3_b32 v164, v162, %6, v164 bitop3:0xbe\nv_bitop3_b32 v164, v163, %7, v164 bitop3:0xbe\n"
      "v_bitop3_b32 v164, v160, %4, v164 bitop3:0xbe\nv_and_b32 v164, 0x3ff, v164\nv_cmp_ne_u32 vcc, 0, v164\ns_and_b64 s[82:83], s[82:83], vcc\n"
      "s_or_b64 s[92:93], s[80:81], s[82:83]\ns_cbranch_scc0 .Le_%=\ns_bcnt1_i32_b64 s96, s[80:81]\ns_mov_b32 s91, s96\ns_bcnt1_i32_b64 s96, s[82:83]\n"
      "s_add_u32 s91, s91, s96\ns_cmp_le_u32 s91, 32\ns_cbranch_scc1 .Lq_%=\ns_sleep 8\n.Lq_%=:\ns_sleep 2\ns_sub_u32 s90, s90, 1\ns_cbranch_scc0 .Lp_%=\n.Le_%=:\n"
      : "+&{v[156:159]}"(v[0]), "+&{v[160:163]}"(v[1])
      : "v"(off[0]), "v"(off[1]), "v"(T[0]), "v"(T[1]), "v"(T[2]), "v"(T[3]), "s"(r)
      : "memory", "vcc", "scc", "v164", "s80", "s81", "s82", "s83", "s88", "s89", "s90", "s92", "s93", "s91", "s96");
}
template <> __device__ __forceinline__ void poll<3, 10, 0, 0>(rsrc_t r, const unsigned (&off)[3], u32x4 T, u32x4 (&v)[3]) {
  asm volatile(
      "s_mov_b64 s[88:89], exec\ns_mov_b32 s90, 0x20000\nv_cmp_ne_u32 vcc, 0x7f000000, %3\ns_mov_b64 s[80:81], vcc\nv_cmp_ne_u32 vcc, 0x7f000000, %4\n"
      "s_mov_b64 s[82:83], vcc\nv_cmp_ne_u32 vcc, 0x7f000000, %5\ns_mov_b64 s[84:85], vcc\n.Lp_%=:\ns_mov_b64 exec, s[80:81]\ns_cbranch_execz .Ls0_%=\n"
      "buffer_load_dwordx4 v[152:155], %3, %10, 0 offen sc1\n.Ls0_%=:\ns_mov_b64 exec, s[82:83]\ns_cbranch_execz .Ls1_%=\n"
      "buffer_load_dwordx4 v[156:159], %4, %10, 0 offen sc1\n.Ls1_%=:\ns_mov_b64 exec, s[84:85]\ns_cbranch_execz .Ls2_%=\n"
      "buffer_load_dwordx4 v[160:163], %5, %10, 0 offen sc1\n.Ls2_%=:\ns_mov_b64 exec, s[88:89]\ns_waitcnt vmcnt(0)\nv_xor_b32 v164, v153, %7\n"
      "v_bitop3_b32 v164, v154, %8, v164 bitop3:0xbe\nv_bitop3_b32 v164, v155, %9, v164 bitop3:0xbe\nv_bitop3_b32 v164, v152, %6, v164 bitop3:0xbe\n"
      "v_and_b32 v164, 0x3ff, v164\nv_cmp_ne_u32 vcc, 0, v164\ns_and_b64 s[80:81], s[80:81], vcc\nv_xor_b32 v164, v157, %7\n"
      "v_bitop3_b32 v164, v158, %8, v164 bitop3:0xbe\nv_bitop3_b32 v164, v159, %9, v164 bitop3:0xbe\nv_bitop3_b32 v164, v156, %6, v164 bitop3:0xbe\n"
      "v_and_b32 v164, 0x3ff, v164\nv_cmp_ne_u32 vcc, 0, v164\ns_and_b64 s[82:83], s[82:83], vcc\ns_or_b64 s[92:93], s[80:81], s[82:83]\nv_xor_b32 v164, v161, %7\n"
      "v_bitop3_b32 v164, v162, %8, v164 bitop3:0xbe\nv_bitop3_b32 v164, v163, %9, v164 bitop3:0xbe\nv_bitop3_b32 v164, v160, %6, v164 bitop3:0xbe\n"
      "v_and_b32 v164, 0x3ff, v164\nv_cmp_ne_u32 vcc, 0, v164\ns_and_b64 s[84:85], s[84:85], vcc\ns_or_b64 s[92:93], s[92:93], s[84:85]\ns_cbranch_scc0 .Le_%=\n"
      "s_sleep 10\ns_sub_u32 s90, s90, 1\ns_cbranch_scc0 .Lp_%=\n.Le_%=:\n"
      : "+&{v[152:155]}"(v[0]), "+&{v[156:159]}"(v[1]), "+&{v[160:163]}"(v[2])
      : "v"(off[0]), "v"(off[1]), "v"(off[2]), "v"(T[0]), "v"(T[1]), "v"(T[2]), "v"(T[3]), "s"(r)
      : "memory", "vcc", "scc", "v164", "s80", "s81", "s82", "s83", "s84", "s85", "s88", "s89", "s90", "s92", "s93");
}
template <> __device__ __forceinline__ void poll<3, 12059650, 0, 0>(rsrc_t r, const unsigned (&off)[3], u32x4 T, u32x4 (&v)[3]) {
  asm volatile(
      "s_mov_b64 s[88:89], exec\ns_mov_b32 s90, 0x20000\nv_cmp_ne_u32 vcc, 0x7f000000, %3\ns_mov_b64 s[80:81], vcc\nv_cmp_ne_u32 vcc, 0x7f000000, %4\n"
      "s_mov_b64 s[82:83], vcc\nv_cmp_ne_u32 vcc, 0x7f000000, %5\ns_mov_b64 s[84:85], vcc\n.Lp_%=:\ns_mov_b64 exec, s[80:81]\ns_cbranch_execz .Ls0_%=\n"
      "buffer_load_dwordx4 v[152:155], %3, %10, 0 offen sc1\n.Ls0_%=:\ns_mov_b64 exec, s[82:83]\ns_cbranch_execz .Ls1_%=\n"
      "buffer_load_dwordx4 v[156:159], %4, %10, 0 offen sc1\n.Ls1_%=:\ns_mov_b64 exec, s[84:85]\ns_cbranch_execz .Ls2_%=\n"
      "buffer_load_dwordx4 v[160:163], %5, %10, 0 offen sc1\n.Ls2_%=:\ns_mov_b64 exec, s[88:89]\ns_waitcnt vmcnt(0)\nv_xor_b32 v164, v153, %7\n"
      "v_bitop3_b32 v164, v154, %8, v164 bitop3:0xbe\nv_bitop3_b32 v164, v155, %9, v164 bitop3:0xbe\nv_bitop3_b32 v164, v152, %6, v164 bitop3:0xbe\n"
      "v_and_b32 v164, 0x3ff, v164\nv_cmp_ne_u32 vcc, 0, v164\ns_and_b64 s[80:81], s[80:81], vcc\nv_xor_b32 v164, v157, %7\n"
      "v_bitop3_b32 v164, v158, %8, v164 bitop3:0xbe\nv_bitop3_b32 v164, v159, %9, v164 bitop3:0xbe\nv_bitop3_b32 v164, v156, %6, v164 bitop3:0xbe\n"
      "v_and_b32 v164, 0x3ff, v164\nv_cmp_ne_u32 vcc, 0, v164\ns_and_b64 s[82:83], s[82:83], vcc\ns_or_b64 s[92:93], s[80:81], s[82:83]\nv_xor_b32 v164, v161, %7\n"
      "v_bitop3_b32 v164, v162, %8, v164 bitop3:0xbe\nv_bitop3_b32 v164, v163, %9, v164 bitop3:0xbe\nv_bitop3_b32 v164, v160, %6, v164 bitop3:0xbe\n"
      "v_and_b32 v164, 0x3ff, v164\nv_cmp_ne_u32 vcc, 0, v164\ns_and_b64 s[84:85], s[84:85], vcc\ns_or_b64 s[92:93], s[92:93], s[84:85]\ns_cbranch_scc0 .Le_%=\n"
      "s_bcnt1_i32_b64 s96, s[80:81]\ns_mov_b32 s91, s96\ns_bcnt1_i32_b64 s96, s[82:83]\ns_add_u32 s91, s91, s96\ns_bcnt1_i32_b64 s96, s[84:85]\ns_add_u32 s91, s91, s96\n"
      "s_cmp_le_u32 s91, 184\ns_cbranch_scc1 .Lq_%=\ns_sleep 4\n.Lq_%=:\ns_sleep 2\ns_sub_u32 s90, s90, 1\ns_cbranch_scc0 .Lp_%=\n.Le_%=:\n"
      : "+&{v[152:155]}"(v[0]), "+&{v[156:159]}"(v[1]), "+&{v[160:163]}"(v[2])
      : "v"(off[0]), "v"(off[1]), "v"(off[2]), "v"(T[0]), "v"(T[1]), "v"(T[2]), "v"(T[3]), "s"(r)
      : "memory", "vcc", "scc", "v164", "s80", "s81", "s82", "s83", "s84", "s85", "s88", "s89", "s90", "s92", "s93", "s91", "s96");
}
template <int STRIDE, int S, int MODE = 0, int K = 0, int N> __device__ __forceinline__ void gather(rsrc_t rws, unsigned off0, u32x4 T, f32x4 (&o)[N]) {
  unsigned off[N];
  u32x4 v[N];
#pragma unroll
  for (int j = 0; j < N; ++j) { off[j] = off0 >= OOB ? OOB : off0 + j * STRIDE; v[j] = T; }
  SCHED();
  poll<N, S, MODE, K>(rws, off, T, v);
  SCHED();
#pragma unroll
  for (int j = 0; j < N; ++j) o[j] = asf4(v[j] ^ T);
}
__device__ __forceinline__ void turn(const u32x4* w, u32x8* o) {
#pragma unroll
  for (int k = 0; k < 4; ++k)
#pragma unroll
    for (int r = 0; r < 8; ++r) { o[2 * k][r] = plo(w[2 * r][k], w[2 * r + 1][k]); o[2 * k + 1][r] = phi(w[2 * r][k], w[2 * r + 1][k]); }
#pragma unroll
  for (int i = 0; i < 8; ++i) asm volatile("" : "+v"(o[i]));
}
constexpr int krow2(int t) { return 32 * (t >> 2) + (t & 3); }
__device__ __forceinline__ void turn2(const u32x4* w, u32x8* o) {
#pragma unroll
  for (int k = 0; k < 4; ++k)
#pragma unroll
    for (int r = 0; r < 4; ++r) { o[k][2 * r] = plo(w[2 * r][k], w[2 * r + 1][k]); o[k][2 * r + 1] = phi(w[2 * r][k], w[2 * r + 1][k]); }
#pragma unroll
  for (int i = 0; i < 4; ++i) asm volatile("" : "+v"(o[i]));
}
__device__ __forceinline__ f32x4 sum8rows(const float* p, int stride) {
  f32x4 t[8];
#pragma unroll
  for (int w = 0; w < 8; ++w) t[w] = *(const f32x4*)(p + w * stride);
  return ((t[0] + t[1]) + (t[2] + t[3])) + ((t[4] + t[5]) + (t[6] + t[7]));
}
template <int S = 2> __device__ __forceinline__ void lds_wait(unsigned* w, unsigned want) {
  POLL_LOOP {
    if (__hip_atomic_load(w, __ATOMIC_RELAXED, __HIP_MEMORY_SCOPE_WORKGROUP) >= want) break;
    if (S) __builtin_amdgcn_s_sleep(S);
  }
  asm volatile("" ::: "memory");
}
__device__ __forceinline__ void lds_post(unsigned* w, int lane, unsigned n = 1u) {
  asm volatile("s_waitcnt lgkmcnt(0)" ::: "memory");
  if (lane == 0) __hip_atomic_fetch_add(w, n, __ATOMIC_RELAXED, __HIP_MEMORY_SCOPE_WORKGROUP);
}

__device__ __forceinline__ void raise(unsigned* w) { asm volatile("" ::: "memory"); __hip_atomic_store(w, 1u, __ATOMIC_RELAXED, __HIP_MEMORY_SCOPE_WORKGROUP); }
template <int S> __device__ __forceinline__ void await(unsigned* w, int n) {
  POLL_LOOP {
    if (__builtin_popcountll(__builtin_amdgcn_ballot_w64(__hip_atomic_load(w, __ATOMIC_RELAXED, __HIP_MEMORY_SCOPE_WORKGROUP) != 0u)) >= n) break;
    if (S) __builtin_amdgcn_s_sleep(S);
  }
  asm volatile("" ::: "memory");
}
__device__ __forceinline__ float score(const u32x4* k, const u32x4* q) {
  float d0 = 0.f, d1 = 0.f;
#pragma unroll
  for (int j = 0; j < 3; ++j) {
    d0 = dot2(k[j][0], q[j][0], d0); d1 = dot2(k[j][1], q[j][1], d1);
    d0 = dot2(k[j][2], q[j][2], d0); d1 = dot2(k[j][3], q[j][3], d1);
  }
  return bfr(bfr(sum8(d0 + d1)) * 0.07216878364870322f);
}
__device__ __forceinline__ void pv8(u32x4 v, unsigned pl, float* acc) {
  const unsigned ph = pl << 16;
#pragma unroll
  for (int k = 0; k < 4; ++k) { acc[2 * k] = dot2(v[k], pl, acc[2 * k]); acc[2 * k + 1] = dot2(v[k], ph, acc[2 * k + 1]); }
}
__device__ __forceinline__ float fold8(const float* acc, int lane) {
  return tr8(tr16(tr32(acc[0], acc[4]), tr32(acc[2], acc[6])), tr16(tr32(acc[1], acc[5]), tr32(acc[3], acc[7])), lane & 8);
}

struct __attribute__((aligned(16))) Lds {
  float ored[NSW][DH];
  float pred[NSW + 1][64];
  float fred[10][128];
  float cred[10][128];
  unsigned short qs[DH];
  unsigned short act[72];
  float ml[NSW][2];
  unsigned tag[4];
  unsigned cnt[4];
  unsigned flag[24];
  unsigned hsum[2][32];
  unsigned near;
  unsigned pad[1];
  u32x4 wst[2][512][8];
};

struct Plan { unsigned w[3 * NWG + 2]; };
int job_of(int h, int s) {
  const int e = (s - h - 1) & 7;
  return h >= NF ? h * 8 + s : e == 7 ? h : e >= 2 ? NF + s * 15 + (h >> 3) * 5 + e - 2 : NF + NP + 2 * h + e;
}
#define UNDEF8(w) asm volatile("" : "=v"(w[0]), "=v"(w[1]), "=v"(w[2]), "=v"(w[3]), "=v"(w[4]), "=v"(w[5]), "=v"(w[6]), "=v"(w[7]))
#define LOFF(x) (unsigned)((const char*)&(x) - (const char*)&L)
#define LAT(T, off) ((T*)((char*)&L + (off)))
typedef const unsigned short* cptr;
typedef const __attribute__((address_space(1))) unsigned short* gptr;
typedef const __attribute__((address_space(4))) unsigned long long* kargs_t;
#define KARGS kargs_t ka = (kargs_t)__builtin_amdgcn_kernarg_segment_ptr(); asm volatile("" : "+s"(ka))
#define ARG(i) ((gptr)ka[i])
__global__ __launch_bounds__(NTHR, 3) void k_fused(cptr, cptr, cptr, cptr, cptr, cptr, cptr, cptr, unsigned short*, unsigned char*, Plan) {
  __shared__ Lds L;
  asm volatile(".Lcode_begin:\n\ts_mul_i32 vcc_lo, %1, 12\n\ts_load_dwordx4 s[96:99], %0, 0x8\n\ts_load_dwordx4 s[92:95], %0, vcc_lo offset:0x50\n\ts_load_dwordx2 s[100:101], %0, 0x18"
               ::"s"(__builtin_amdgcn_kernarg_segment_ptr()), "s"(blockIdx.x) : "vcc", "s92", "s93", "s94", "s95", "s96", "s97", "s98", "s99", "s100", "s101");
  SCHED();
  const unsigned tid0 = __builtin_amdgcn_readfirstlane(threadIdx.x);
  int wave = tid0 >> 6;
  {
    unsigned t;
    asm volatile("s_or_b32 s88, %1, %2\n\ts_cmp_lt_u32 s88, %3\n\ts_cbranch_scc0 1f\n\ts_getpc_b64 s[88:89]\n\ts_mov_b32 m0, %4\n\tv_lshlrev_b32 %0, 7, %5\n\ts_andn2_b32 s88, s88, 0xff\n\ts_and_b32 s89, s89, 0xffff\n\t"
                 "s_mov_b32 s90, .Lcode_end-.Lcode_begin\n\ts_max_u32 s90, s90, .Lcode_end2-.Lcode_begin\n\ts_max_u32 s90, s90, .Lcode_end3-.Lcode_begin\n\ts_mov_b32 s91, 0x20000\n\t"
                 "s_movk_i32 s87, 0x2000\n\tbuffer_load_dword %0, s[88:91], 0 offen lds\n\tbuffer_load_dword %0, s[88:91], s87 offen lds\n\ts_movk_i32 s87, 0x4000\n\t"
                 "buffer_load_dword %0, s[88:91], s87 offen lds\n1:"
                 : "=&v"(t) : "s"(blockIdx.x), "s"(tid0), "n"(CPF), "n"(0), "v"(threadIdx.x) : "memory", "scc", "s87", "s88", "s89", "s90", "s91");
    SCHED();
  }
  u32x4 first[6];
  gptr kc0, vc0, wq;
  unsigned kvo, plan, vq;
  unsigned long long tk0;
  {
    const bool st = wave < NSW;
    unsigned so[6], idx = (threadIdx.x >> 3) & 63u, c16 = (threadIdx.x & 7u) * 16u, size = st ? 32u * SK * DH * 2u : D * 6144u * 2u;
#pragma unroll
    for (int i = 1; i < 6; ++i) so[i] = st ? (i % 3) * 128 + (i / 3) * 24576 : i * 12288;
    asm volatile("" : "+s"(so[1]), "+s"(so[2]), "+s"(so[3]), "+s"(so[4]), "+s"(so[5]), "+s"(size));
    asm volatile("" : "+v"(idx), "+v"(c16));
    gptr p;
    asm volatile("s_cmp_lt_u32 %3, 8\n\ts_waitcnt lgkmcnt(0)\n\ts_cselect_b32 s93, s92, s93\n\tv_add_u32 %1, s93, %4\n\ts_cselect_b64 %0, s[96:97], s[100:101]\n\t"
                 "s_cselect_b32 s95, 0, s94\n\ts_bfe_u32 s95, s95, 0x4000a\n\ts_mul_i32 s95, s95, 0x3000\n\ts_max_u32 s95, s95, 0x180\n\tv_mad_u32_u24 %1, %2, s95, %1"
                 : "=&s"(p), "=&v"(vq) : "v"(idx), "s"(wave), "v"(c16) : "scc", "s93", "s95");
    const rsrc_t r = mk(p, size);
#pragma unroll
    for (int i = 0; i < 6; ++i) {
      if (i == 0) { SCHED(); tk0 = __builtin_amdgcn_s_memrealtime() - TZ; SCHED(); }
      first[i] = ld128<AUX_NT>(r, vq, i ? so[i] : 0u);
    }
    SCHED();
    asm volatile("s_mov_b64 %0, s[96:97]\n\ts_mov_b64 %1, s[98:99]\n\ts_mov_b64 %2, s[100:101]\n\ts_mov_b32 %3, s92\n\ts_mov_b32 %4, s94" : "=s"(kc0), "=s"(vc0), "=s"(wq), "=s"(kvo), "=s"(plan));
  }
  int lane = threadIdx.x & 63, b = blockIdx.x;
  asm volatile("" : "+v"(lane));
  asm volatile("" : "+s"(wave), "+s"(b), "+s"(plan));
  const int h = b >> 3, s = b & 7;
  const int g = lane >> 3, c = lane & 7, tk = plan >> 16;
  const bool stream = wave < NSW, isF = tk < NF;
  const int a = wave - NSW;
  u32x2 e1 = {0u, 0u};
  u32x4 T = {0u, 0u, 0u, 0u};
  u32x4 wt[8];

  if (__builtin_expect(stream, 1)) {
    if (wave < 2) {
      const unsigned t = wave * 64 + lane, i = t - 12u;
      if (t < 94u) L.cnt[t] = i < 16u ? i >= 10u : i < 80u && ((i - 16u) & 31u) < 20u ? ~0u : 0u;
    }
    const int nk = plan & 1023;
    const unsigned voff = vq - kvo;
    const rsrc_t rk = mk(kc0 + kvo / 2, nk * 384), rv = mk(vc0 + kvo / 2, nk * 384);
    u32x4 kb[27], vb[27];
    unsigned due, tau, tau8;
#pragma unroll
    for (int i = 0; i < 27; ++i) {
      if (i == BA) bar();
      if (i == KW) {
        const bool nine = 512 + 8 * wave < nk;
        tau = isF ? 0u : tk < NF + NP ? (nine ? 16 * (SP - SA) / 53 : 16 * (SP - SA) / 47) : nine ? 16 * (SE - SA) / 53 : 16 * (SE - SA) / 47;
        tau8 = nine ? tau : 0u;
        due = ((unsigned)tk0 << 4) + (isF ? -65536 : 16 * SA) + (KW - 1) * tau;
        asm volatile("s_memrealtime s[92:93]" : "+s"(tau), "+s"(tau8) :: "s92", "s93");
      }
      if (i >= KW) PACE(KW, i < 24 ? tau : tau8);
      kb[i] = i < 6 ? first[i] : ld128<AUX_NT>(rk, voff + (i % 3) * 128, (i / 3) * 24576);
      SCHED();
    }
#define LISTLOAD(n) vb[n] = ld128<AUX_NT>(rv, voff + ((n) % 3) * 128, (8 - (n) / 3) * 24576)
#pragma unroll
    for (int n = 0; n < VW; ++n) {
      PACE(KW, n < 3 ? tau8 : tau);
      LISTLOAD(n);
      SCHED();
    }
    lds_wait(&L.cnt[1], 1);
    u32x4 qv[3];
#pragma unroll
    for (int j = 0; j < 3; ++j) qv[j] = *(const u32x4*)&L.qs[j * 64 + c * 8];
    float sc[9];
#pragma unroll
    for (int t = 0; t < 9; ++t) {
      sc[t] = score(kb + 3 * t, qv);
      if (t & 1) {
        SCHED();
#pragma unroll
        for (int n = VW + VN * (t - 1) / 8; n < VW + VN * (t + 1) / 8; ++n) LISTLOAD(n);
        SCHED();
      }
    }
    due += VN * tau;
    asm volatile("s_memrealtime s[92:93]" : "+s"(due) :: "s92", "s93");
    constexpr int V1 = VW + VN;
#pragma unroll
    for (int t = 1; t < 9; ++t) if (64 * t + 8 * wave >= nk) sc[t] = -__builtin_inff();
    float m = sc[0];
#pragma unroll
    for (int t = 1; t < 9; ++t) m = fmaxf(m, sc[t]);
    m = maxg(m);
    float lsum = 0.f;
    unsigned pp[9];
#pragma unroll
    for (int t = 0; t < 9; ++t) { const float p = __builtin_amdgcn_exp2f((sc[t] - m) * L2E); lsum += p; pp[t] = pk(p, 0.f); }
    lsum = sumg(lsum);
    if (lane == 0) *(f32x2*)L.ml[wave] = f32x2{m, lsum};
    lds_post(&L.cnt[0], lane);
    float acc[24];
    unsigned odst = LOFF(L.ored[wave][8 * c + g]);
    KARGS;
    const rsrc_t rw = mk(isF ? ARG(7) : ARG(4), isF ? FF * D * 2 : tk < NF + NP ? 6144 * D * 2 : 0);
    unsigned wvo = (unsigned)(isF ? klane(lane) + (lane & 8) : (lane >> 4) + (lane & 8)) * 1280u + c * 16u;
    int ws0 = isF ? 64 * tk * 1280 + wave * 128 : ((tk - NF) / 10 * 512 + 64 * wave) * 1280 + (tk - NF) % 10 * 128, ws1 = isF ? 1280 : 4 * 1280, ws2 = isF ? 2 * 1280 : 16 * 1280;
    const rsrc_t rws = mk(ARG(9), WS_END);
    unsigned oo = WS_O - 1u + (unsigned)g * O_ROW + orow((unsigned)((tk - NF) / 10 * 512 + 64 * wave + 4 * c) * 4u);
    asm volatile("" : "+v"(odst), "+v"(wvo), "+v"(oo));
    asm volatile("" : "+s"(ws0), "+s"(ws1), "+s"(ws2));
#define WLOAD(t) wt[t] = ld128<AUX_NT>(rw, wvo, ws0 + ((t) & 1) * ws1 + (((t) >> 1) & 1) * ws2 + ((t) >> 2) * 32 * 1280)
#pragma unroll
    for (int e = 0; e < 24; ++e) acc[e] = 0.f;
#pragma unroll
    for (int n = 0; n < 27; ++n) {
      if (n + VP >= V1 && n + VP < 27) { PACE(VP, tau); LISTLOAD(n + VP); }
      SCHED();
      pv8(vb[n], pp[8 - n / 3], acc + 8 * (n % 3));
      if (n + WW - WD >= 27 && n < 26) { SCHED(); WLOAD(n + WW - WD - 27); SCHED(); }
      if (n >= 24) *LAT(float, odst + (n % 3) * 256) = fold8(acc + 8 * (n % 3), lane);
      SCHED();
    }
    raise(&L.flag[wave]);
    if (isF) {
#pragma unroll
      for (int t = WW - WD > 0 ? WW - WD - 1 : 0; t < 8; ++t) { VMWAIT(W2 - 1); WLOAD(t); SCHED(); }
    } else if (tk < NF + NP) {
      T = *(const u32x4*)L.tag;
      lds_wait(&L.near, 1);
      oo += L.near;
      unsigned off[2] = {oo, oo + 128u}, pdst = LOFF(L.pred[g == 0 ? wave : NSW][8 * c]);
      int idx = smf_idx(lane & 1);
      asm volatile("" : "+v"(pdst), "+v"(idx), "+v"(off[0]), "+v"(off[1]), "+v"(T));
      f32x4 d[4];
      zero(d);
      SCHED();
#pragma unroll
      for (int t = WW - WD > 0 ? WW - WD - 1 : 0; t < 8; ++t) {
        VMWAIT(W2 - 1);
        if (SW) while ((int)((unsigned)__builtin_amdgcn_s_memrealtime() - ((unsigned)tk0 + SW - (7 - t) * TW)) < 0) __builtin_amdgcn_s_sleep(1);
        WLOAD(t);
        SCHED();
      }
      VMWAIT(PL);
      u32x4 v[2] = {ld128<AUX_SC1>(rws, off[0], 0), ld128<AUX_SC1>(rws, off[1], 0)};
      SCHED();
      u32x8 wp[4];
      turn2(wt, wp);
      SCHED();
      poll<2, OS + 256 * OL + 65536 * OK, 5, 0>(rws, off, T, v);
      SCHED();
      float o8[8];
#pragma unroll
      for (int i = 0; i < 8; ++i) o8[i] = asf(v[i >> 2][i & 3] ^ T[i & 3]);
      const float o1 = fold8(o8, lane);
      const unsigned op = pk(dpp<0xA0>(o1), dpp<0xF5>(o1));
      const u32x4 av = {op, shl<4>(op), shl<8>(op), shl<12>(op)};
#pragma unroll
      for (int k = 0; k < 4; ++k) { d[k] = smf4(av, wp[k], d[k], idx); *LAT(f32x2, pdst + 8 * k) = f32x2{d[k][0] + dpp<ROR8>(d[k][2]), d[k][1] + dpp<ROR8>(d[k][3])}; }
      bar();
      LEAVE();
    } else {
      LEAVE();
    }
  } else if (isF && a < 2) {
    __builtin_amdgcn_s_setprio(DP);
    KARGS;
    const rsrc_t rw = mk((a ? ARG(6) : ARG(5)) + 64 * h, D * FF * 2 - 128 * h);
    const unsigned vo = (unsigned)g * 3072u + c * 16u;
    unsigned so = 0u, dst = LOFF(L.wst[a]) + (unsigned)(size_t)&L;
#define DMA() asm volatile("s_mov_b32 m0, %1\n\ts_add_u32 %1, %1, 0x400\n\tbuffer_load_dwordx4 %2, %3, %0 offen nt lds\n\ts_add_u32 %0, %0, 0x6000" : "+s"(so), "+s"(dst) : "v"(vo), "s"(rw) : "memory", "scc")
#pragma nounroll
    for (int i = 0; i < DB; ++i) { VMWAIT(DW - 1); DMA(); }
    bar();
#pragma nounroll
    for (int i = DB; i < 64; ++i) { VMWAIT(DW - 1); DMA(); }
    __builtin_amdgcn_s_setprio(3);
    asm volatile(".Lcode_end3:");
    UNDEF8(wt);
  } else {
    __builtin_amdgcn_s_setprio(3);
    if (AB == 2) bar();
    const int nl = (plan >> 10) & 15, qpart = (plan >> 14) & 3;
    const bool big = nl > 8, qjob = nl != 0;
    constexpr int NS = 8;
    const rsrc_t rq = mk(wq, D * 6144 * 2);
    u32x4 wb[NB];
#pragma unroll
    for (int i = 0; i < NS; ++i) wb[i] = i < 6 ? first[i] : ld128<AUX_NT>(rq, nl > 6 ? vq : OOB, i * 12288);
    if (big) {
#pragma unroll
      for (int i = NS; i < NB; ++i) wb[i] = ld128<AUX_NT>(rq, vq, i * 12288);
    }
    const unsigned row = vq / 12288u, qline = __builtin_amdgcn_readfirstlane((vq - row * 12288u) >> 7), xo = qjob ? row * 2u : OOB;
    SCHED();
    KARGS;
    const rsrc_t rws = mk(ARG(9), WS_END), rx = mk(ARG(0), D * 2);
    const u32x4 xs4 = ld128<0>(rx, (unsigned)lane * 16u, 0);
    const unsigned xs1 = ld32<0>(rx, 1024u + (unsigned)lane * 4u, 0);
    const u32x4 xr0 = ld128<0>(rx, xo, 0), xr4 = ld128<0>(rx, xo + 16u, 0);
    const u32x2 ep = ld64<AUX_SC1>(rws, WS_EPOCH, 0);
    unsigned pf[3] = {0u, 0u, 0u};
    if (a == 3) {
      pf[0] = ld32<0>(mk(ARG(8), D * 2), 0, 0);
      const unsigned kvn = (plan & 1023u) * 384u, po = lane == 0 ? 0u : lane == 1 ? kvn - 128u : OOB;
      pf[1] = ld32<AUX_NT>(mk(vc0 + kvo / 2, kvn), po, 0);
      pf[2] = ld32<AUX_NT>(mk(kc0 + kvo / 2, kvn), lane == 1 ? po : OOB, 0);
    }
    if (AB == 0) bar();
    f32x4 qp = {0.f, 0.f, 0.f, 0.f};
    if (qjob) {
      const unsigned xs[5] = {xs4[0], xs4[1], xs4[2], xs4[3], xs1}, xr[7] = {xr0[0], xr0[1], xr0[2], xr0[3], xr4[0], xr4[1], xr4[2]};
      float sm = 0.f;
#pragma unroll
      for (int i = 0; i < 5; ++i) sm += lo16(xs[i]) + hi16(xs[i]);
      const float mean = sum64(sm) * (1.f / D);
      float sq = 0.f;
#pragma unroll
      for (int i = 0; i < 5; ++i) { const float d0 = lo16(xs[i]) - mean, d1 = hi16(xs[i]) - mean; sq += d0 * d0 + d1 * d1; }
      const float mb = bfr(mean), rden = rstd(bfr(sum64(sq) * (1.f / D)));
      float qa[8];
#pragma unroll
      for (int k = 0; k < 8; ++k) qa[k] = 0.f;
      auto rows = [&](int i) {
        const unsigned xp = pk(bfr(lo16(xr[i]) - mb) * rden, bfr(hi16(xr[i]) - mb) * rden);
#pragma unroll
        for (int k = 0; k < 4; ++k) {
          qa[2 * k] = dot2(plo(wb[2 * i][k], wb[2 * i + 1][k]), xp, qa[2 * k]);
          qa[2 * k + 1] = dot2(phi(wb[2 * i][k], wb[2 * i + 1][k]), xp, qa[2 * k + 1]);
        }
      };
#pragma unroll
      for (int i = 0; i < NS / 2; ++i) rows(i);
      if (big) {
#pragma unroll
        for (int i = NS / 2; i < NB / 2; ++i) rows(i);
      }
#pragma unroll
      for (int i = 0; i < 4; ++i) { float t = tr32(qa[i], qa[i + 4]); t = tr16(t, t); qp[i] = t + dpp<ROR8>(t); }
    }
    if (AB == 1) bar();
    if ((lane & 24) == 0) *(f32x4*)&L.pred[a][c * 8 + (lane >> 5) * 4] = qp;
    lds_post(&L.cnt[3], lane);
    const unsigned long long en = (((unsigned long long)ep[1] << 32) | ep[0]) + 1ull;
    e1 = u32x2{(unsigned)en, (unsigned)(en >> 32)};
    T = make_tag(e1);
    if (a == 0 && qjob) {
      lds_wait(&L.cnt[3], 4);
      const float* src = &L.pred[0][4 * (lane & 15)];
      qp = (*(const f32x4*)src + *(const f32x4*)(src + 64)) + (*(const f32x4*)(src + 128) + *(const f32x4*)(src + 192));
      const unsigned ex = asu(fmaxf(fmaxf(fabsf(qp[0]), fabsf(qp[1])), fmaxf(fabsf(qp[2]), fabsf(qp[3])))) >> 23;
      u32x4 qo;
#pragma unroll
      for (int i = 0; i < 4; ++i) qo[i] = ((unsigned)min((int)__builtin_rintf(__builtin_ldexpf(qp[i], 145 - (int)ex)), 524287) << 12) | (((ex >> (2 * i)) & 3u) << 10) | T[i];
      st128(qo, rws, lane < 16 ? WS_Q + (unsigned)qline * 1024u + qpart * 256u + lane * 16u : OOB);
    } else if (a == 2) {
      unsigned off[3];
      u32x4 v[3];
#pragma unroll
      for (int j = 0; j < 3; ++j) {
        const int ql = 3 * h + j;
        off[j] = lane < (ql >= 73 || (ql & 7) == 3 ? 48 : 32) ? WS_Q + (unsigned)ql * 1024u + lane * 16u : OOB;
        v[j] = T;
      }
      while (__builtin_amdgcn_s_memrealtime() < tk0 + QT) __builtin_amdgcn_s_sleep(4);
      SCHED();
      poll<3, QS, 0, 0>(rws, off, T, v);
      SCHED();
#pragma unroll
      for (int j = 0; j < 3; ++j) {
        const int ex = (int)(((v[j][0] >> 10) & 3u) | ((v[j][1] >> 8) & 12u) | ((v[j][2] >> 6) & 48u) | ((v[j][3] >> 4) & 192u)) - 145;
        f32x4 qf;
#pragma unroll
        for (int i = 0; i < 4; ++i) { float t = __builtin_ldexpf((float)((int)v[j][i] >> 12), ex); t = tr16(t, t); qf[i] = tr32(t, t); }
        if (lane < 16) *(u32x2*)&L.qs[j * 64 + lane * 4] = u32x2{pk(qf[0], qf[1]), pk(qf[2], qf[3])};
      }
      if (lane == 0) *(u32x4*)L.tag = T;
      lds_post(&L.cnt[1], lane);
    } else if (a == 3) {
      st128(T, rws, (s == (h & 7) && lane < 32) ? WS_PR + (unsigned)h * 256u + (lane & 15) * 16u + (lane >> 4) * TWIN : OOB);
      asm volatile("" ::"v"(pf[0]), "v"(pf[1]), "v"(pf[2]));
    }
    asm volatile(".Lcode_end2:");
    UNDEF8(wt);
  }

  const int wave0 = wave, tk1 = tk;
  {
    int lane = threadIdx.x & 63, wave = wave0, b = blockIdx.x, tk = tk1;
    asm volatile("" : "+v"(lane));
    asm volatile("" : "+s"(wave), "+s"(b), "+s"(tk));
    const int g = lane >> 3, c = lane & 7, h = b >> 3, s = b & 7, a = wave - NSW;
    const bool stream = wave < NSW;
    KARGS;
    const gptr w1 = ARG(5), w3 = ARG(6), w2 = ARG(7), out = ARG(8), ws = ARG(9);
    const rsrc_t rws = mk(ws, WS_END), rx = mk(ARG(0), D * 2);
    auto near = [&]() { lds_wait(&L.near, 1); return (unsigned)__builtin_amdgcn_readfirstlane(L.near) - 1u; };
    float fw = 0.f;
    if (a == 3) {
      const unsigned half = (__builtin_amdgcn_s_getreg(20 | (3 << 11)) >> 2) & 1u;
      while (__builtin_amdgcn_s_memrealtime() < tk0 + PT) __builtin_amdgcn_s_sleep(8);
      unsigned tt[2] = {~0u, ~0u};
#pragma unroll
      for (int r = 0; r < 4; ++r) {
        unsigned long long t0, t1;
        unsigned x;
        asm volatile("s_memtime %0\n\ts_waitcnt lgkmcnt(0)\n\ts_load_dword %2, %3, %4\n\ts_waitcnt lgkmcnt(0)\n\ts_memtime %1\n\ts_waitcnt lgkmcnt(0)"
                     : "=&s"(t0), "=&s"(t1), "=&s"(x) : "s"(ws + (WS_PR + h * 256) / 2), "n"((r >> 1) * 128 + (r & 1) * TWIN) : "memory");
        tt[r & 1] = min(tt[r & 1], (unsigned)(t1 - t0));
      }
      const int dt = max(-256, min(256, (int)(tt[0] - tt[1])));
      lds_wait(&L.cnt[0], NSW);
      const f32x2 mlw = *(const f32x2*)L.ml[c];
      const float mwg = asf(asu(max8(mlw[0])) & ~0x3FFu);
      const float lwg = sum8(mlw[1] * __builtin_amdgcn_exp2f((mlw[0] - mwg) * L2E));
      const u32x4 own = tagged(f32x4{mwg, lwg, (float)(half ? -dt : dt), 0.f}, T);
      st128(own, rws, (lane == 0 ? WS_ML + (unsigned)b * 128u : OOB));
      f32x4 mo[1];
      gather<0, MS>(rws, ((lane < 8 && lane != s) ? WS_ML + (unsigned)(h * 8 + lane) * 128u : OOB), T, mo);
      if (lane == s) mo[0] = asf4(own ^ T);
      const unsigned pol = sum8(mo[0][2]) > 0.f;
      if (lane == 0) __hip_atomic_store(&L.near, 1u + ((pol ^ half) & 1u) * TWIN, __ATOMIC_RELAXED, __HIP_MEMORY_SCOPE_WORKGROUP);
      const float mh = max8(mo[0][0]);
      fw = __builtin_amdgcn_exp2f((mlw[0] - mh) * L2E) * __builtin_amdgcn_rcpf(sum8(mo[0][1] * __builtin_amdgcn_exp2f((mo[0][0] - mh) * L2E)));
    }
    if (a == 3) {
      const int dq = min(lane, 47);
      unsigned src = LOFF(L.ored[0][4 * dq]);
      unsigned so = (lane < 48 ? WS_O + (unsigned)s * O_ROW + orow((unsigned)h * (DH * 4) + lane * 16u) : OOB);
      float f[NSW];
#pragma unroll
      for (int w = 0; w < NSW; ++w) { f[w] = rdl(fw, w); asm volatile("" : "+s"(f[w])); }
      asm volatile("" : "+v"(src), "+v"(so));
      await<2>(&L.flag[c], 48);
      f32x4 t[NSW];
      POLL_LOOP {
        const unsigned fl = __hip_atomic_load(&L.flag[c], __ATOMIC_RELAXED, __HIP_MEMORY_SCOPE_WORKGROUP);
        asm volatile("" ::: "memory");
#pragma unroll
        for (int w = 0; w < NSW; ++w) t[w] = *LAT(const f32x4, src + w * DH * 4);
        asm volatile("" ::: "memory");
        SCHED();
        if (__builtin_amdgcn_ballot_w64(fl == 0u) == 0ull) break;
      }
#pragma unroll
      for (int w = 0; w < NSW; ++w) t[w] *= f[w];
      const f32x4 o = ((t[0] + t[1]) + (t[2] + t[3])) + ((t[4] + t[5]) + (t[6] + t[7]));
      const u32x4 ot = tagged(o, T);
      st128(ot, rws, so);
      st128(ot, rws, so + TWIN);
    }
    if (tk < NF) {
      if (wave < 10) {
        u32x4 wb[16];
        unsigned xv;
        {
          const rsrc_t rw2 = mk(w2, FF * D * 2);
          xv = __builtin_amdgcn_raw_buffer_load_b16(rx, (unsigned)(64 * wave + hrow(lane)) * 2u, 0, 0);
          if (!stream) {
            const gptr pw = ((lane & 8) ? w3 : w1) + (64 * wave + hrow(klane(lane))) * FF + 64 * tk + 8 * c;
#pragma unroll
            for (int t = 0; t < 16; ++t) {
              VMWAIT(DW - 1);
              wb[t] = __builtin_nontemporal_load((const __attribute__((address_space(1))) u32x4*)(pw + hrow(krow(t)) * FF));
              SCHED();
            }
            VMWAIT(16);
            lds_post(&L.cnt[3], lane, 256u);
            await<8>(&L.flag[c], 8);
#pragma unroll
            for (int t = 0; t < 8; ++t) {
              VMWAIT(WW - 1);
              wt[t] = ld128<AUX_NT>(rw2, (unsigned)(klane(lane) + (lane & 8)) * 1280u + c * 16u, (64 * tk + krow2(t)) * 1280 + wave * 128);
              SCHED();
            }
          }
          if (stream) {
            lds_wait(&L.cnt[3], 512);
#pragma unroll
            for (int t = 0; t < 16; ++t) wb[t] = L.wst[(lane >> 3) & 1][64 * wave + hrow(klane(lane)) + hrow(krow(t))][c];
          }
        }
        u32x8 b1[8], b2[4];
        turn(wb, b1); turn2(wt, b2);
        SCHED();
        T = *(const u32x4*)L.tag;
        int idx = smf_idx(lane & 8), idx2 = smf_idx(lane & 1);
        unsigned fdst = LOFF(L.fred[wave][(lane >> 5) * 64 + 2 * c + (g & 1)]);
        {
          const u32x4 ones = {0x3F803F80u, 0x3F803F80u, 0x3F803F80u, 0x3F803F80u};
#pragma unroll
          for (int i = 0; i < 4; ++i) *LAT(float, fdst + (LOFF(L.cred) - LOFF(L.fred)) + i * 64) = tr32(smf(ones, b1[i], idx), smf(ones, b1[i + 4], idx));
        }
        raise(&L.flag[8 + wave]);
        f32x4 pv[3];
        float xf = lo16(xv);
        if (lane & 16) fdst = LOFF(L.pred);
        unsigned hdst = (lane == 0 || lane == 40) ? LOFF(L.hsum[0][2 * wave + (g & 1)]) : LOFF(L.pred), asrc = LOFF(L.act[(lane & 48) + 4 * (lane & 2)]);
        unsigned as0 = lane & 8 ? LOFF(L.act[64]) : asrc, as1 = lane & 8 ? asrc : LOFF(L.act[64]);
        unsigned so = ((lane & 8) == 0 ? WS_P3 + prow(tk) + (lane & 16) * (TWIN / 16) + (unsigned)wave * 256u + (lane >> 5) * 128u + c * 16u : OOB);
        asm volatile("" : "+v"(fdst), "+v"(hdst), "+v"(xf), "+v"(as0), "+v"(as1), "+v"(so), "+v"(idx), "+v"(idx2));
        f32x4 d[8], e[2];
        zero(d);
        u32x4 ones = {0x3F803F80u, 0x3F803F80u, 0x3F803F80u, 0x3F803F80u};
        asm volatile("" : "+v"(ones));
        gather<32768, PS + 256 * PLS + 65536 * PK>(rws, WS_P2 + near() + prow(lane >> 4) + (unsigned)wave * 256u + (unsigned)(lane & 15) * 16u, T, pv);
        {
          const f32x4 ps = (pv[0] + pv[1]) + pv[2];
          const float at = tr16(tr32(ps[0], ps[2]), tr32(ps[1], ps[3]));
          const float hv = bfr(xf + bfr(at));
          const unsigned hp = phi(asu(hv), asu(dpp<XOR1>(hv)));
          const u32x4 a0 = {hp, shl<2>(hp), shl<4>(hp), shl<6>(hp)};
          const float s1 = mfma(a0, ones), s2 = mfma(a0, a0);
          SCHED();
#pragma unroll
          for (int i = 0; i < 2; ++i) d[i] = smf4(a0, b1[(i >> 1) + 4 * (i & 1)], d[i], idx);
          SCHED();
          *LAT(float, hdst) = s1; *LAT(float, hdst + 128) = s2;
          SCHED();
#pragma unroll
          for (int i = 2; i < 8; ++i) d[i] = smf4(a0, b1[(i >> 1) + 4 * (i & 1)], d[i], idx);
#pragma unroll
          for (int i = 0; i < 4; ++i) *LAT(float, fdst + i * 64) = tr32(d[2 * i][0], d[2 * i + 1][0]);
        }
        bar();
        zero(e);
        bar();
        const u32x4 a0 = *LAT(const u32x4, as0), a1 = *LAT(const u32x4, as1);
        f32x4 d0 = smf4(a0, b2[0], e[0], idx2), d1 = smf4(a0, b2[1], e[1], idx2);
        SCHED();
        d0 = smf4(a1, b2[2], d0, idx2); d1 = smf4(a1, b2[3], d1, idx2);
        st128(tagged(f32x4{d0[0] + dpp<ROR8>(d0[2]), d0[1] + dpp<ROR8>(d0[3]), d1[0] + dpp<ROR8>(d1[2]), d1[1] + dpp<ROR8>(d1[3])}, T), rws, so);
        LEAVE();
      }
    } else if (a == 3 && tk >= NF + NP && tk < NF + NP + NR) {
      const int j = tk - NF - NP, col0 = 64 * (j >> 3) + 16 * (j & 3) + (j & 4) + 8 * (lane & 1), e = 2 * (lane >> 5) + ((lane >> 4) & 1);
      const unsigned xv = __builtin_amdgcn_raw_buffer_load_b16(rx, (unsigned)(col0 + e) * 2u, 0, 0);
      const unsigned fo = near() + prow(lane >> 1);
      auto fold = [&](f32x4 f) {
        const float r = tr16(tr32(f[0], f[2]), tr32(f[1], f[3]));
        return sum21(r + dpp<ROR8>(r));
      };
      f32x4 pv[1];
      if (G3) {
        while (__builtin_amdgcn_s_memrealtime() < tk0 + G3) __builtin_amdgcn_s_sleep(2);
        const unsigned x = ld32<AUX_SC1>(rws, lane < 48 ? WS_P3 + fo + (unsigned)j * 32u : OOB, 0);
        asm volatile("" ::"v"(x));
      }
      if (GR) while (__builtin_amdgcn_s_memrealtime() < tk0 + GR) __builtin_amdgcn_s_sleep(2);
      gather<0, HS>(rws, (lane < 24 ? WS_P2 + fo + (unsigned)col0 * 4u : OOB), T, pv);
      float hsel = bfr(lo16(xv) + bfr(fold(pv[0])));
      unsigned so = ((lane & 14) == 0 ? (unsigned)(col0 + e) * 2u : OOB), se = ((tk == NF + NP && lane == 0) ? WS_EPOCH : OOB);
      asm volatile("" : "+v"(hsel), "+v"(so), "+v"(se));
      gather<0, RS>(rws, (lane < 48 ? WS_P3 + fo + (unsigned)j * 32u + (lane & 1) * 16u : OOB), T, pv);
      __builtin_amdgcn_raw_buffer_store_b16((unsigned short)(pk(hsel + bfr(fold(pv[0])), 0.f) & 0xFFFFu), mk(out, D * 2), so, 0, AUX_SC1 | 1);
      st64(e1, rws, se);
      LEAVE();
    }
    if (a == 2) {
      if (tk < NF) {
        const int li = c * 16 + g * 2;
        if (lane < 4) *(unsigned*)&L.act[64 + 2 * lane] = 0u;
        await<2>(&L.flag[8 + (lane & 15)], 64);
        f32x2 cs = {0.f, 0.f};
#pragma unroll
        for (int j = 0; j < 10; ++j) cs += *(const f32x2*)&L.cred[j][li];
        unsigned src = LOFF(L.fred[0][li]), adst = LOFF(L.act[lane]);
        asm volatile("" : "+v"(cs), "+v"(src), "+v"(adst));
        unsigned* const hs = &L.hsum[0][lane];
        POLL_LOOP {
          if (__builtin_popcountll(__builtin_amdgcn_ballot_w64(__hip_atomic_load(hs, __ATOMIC_RELAXED, __HIP_MEMORY_SCOPE_WORKGROUP) != ~0u)) >= 28) break;
          __builtin_amdgcn_s_sleep(2);
        }
        unsigned sv, r0, r1, r2, r3, cap = SPIN_CAP;
#define LOOK(N, O) "ds_read_b32 %" #N ", %6\n\ts_waitcnt lgkmcnt(3)\n\tv_cmp_eq_u32 vcc, -1, %" #O "\n\tv_mov_b32 %0, %" #O "\n\ts_cbranch_vccz 2f\n\t"
        asm volatile("ds_read_b32 %1, %6\n\tds_read_b32 %2, %6\n\tds_read_b32 %3, %6\n"
                     "1:\n\t" LOOK(4, 1) LOOK(1, 2) LOOK(2, 3) LOOK(3, 4) "s_sub_u32 %5, %5, 1\n\ts_cbranch_scc0 1b\n"
                     "2:"
                     : "=&v"(sv), "=&v"(r0), "=&v"(r1), "=&v"(r2), "=&v"(r3), "+s"(cap) : "v"(LOFF(*hs) + (unsigned)(size_t)&L) : "vcc", "scc", "memory");
        SCHED();
        asm volatile("s_barrier" ::: "memory");
        SCHED();
        f32x2 t[10];
#pragma unroll
        for (int j = 0; j < 10; ++j) t[j] = *LAT(const f32x2, src + j * 512);
        SCHED();
        float s1 = sum8(asf(sv));
        s1 += dpp<ROR8>(s1);
        s1 = tr16(s1, s1);
        const auto s12 = __builtin_amdgcn_permlane32_swap(asu(s1), asu(s1), false, false);
        const float mean = asf(s12[0]) * (1.f / D), s2 = asf(s12[1]);
        float rden = rstd(bfr(fmaxf(s2 * (1.f / D) - mean * mean, 0.f)));
        cs *= -bfr(mean);
        asm volatile("" : "+v"(cs), "+v"(rden));
        SCHED();
        const f32x2 gu = ((((t[0] + t[1]) + (t[2] + t[3])) + ((t[4] + t[5]) + (t[6] + t[7]))) + ((t[8] + t[9]) + cs)) * rden;
        asm volatile("" ::"v"(r0), "v"(r1), "v"(r2), "v"(r3), "v"(gu));
        const unsigned gp = pk(gu[0], gu[1]);
        const float gt = lo16(gp);
        *LAT(unsigned short, adst) = (unsigned short)(pk(bfr(gt * __builtin_amdgcn_rcpf(1.f + __builtin_amdgcn_exp2f(-gt * L2E))) * hi16(gp), 0.f) & 0xFFFFu);
        bar();
        LEAVE();
      }
      if (tk >= NF && tk < NF + NP) {
        unsigned src = LOFF(L.pred[0][4 * (lane & 15)]);
        unsigned so = (lane < 32 ? WS_P2 + prow((tk - NF) / 10) + (lane & 16) * (TWIN / 16) + (unsigned)((tk - NF) % 10) * 256u + (lane & 15) * 16u : OOB);
        asm volatile("" : "+v"(src), "+v"(so));
        bar();
        st128(tagged(sum8rows(LAT(const float, src), 64), T), rws, so);
        LEAVE();
      }
    }
    if (!stream && a != 2) {
      if (tk < NF) { if (a == 3) { bar(); bar(); } }
      else if (tk < NF + NP) bar();
      LEAVE();
    }
  }
  asm volatile(".Lcode_end:");
}

}

extern "C" void kernel_launch(void* const* d_in, const int* in_sizes, int n_in, void* d_out, int out_size, void* d_ws, size_t ws_size,
                              hipStream_t stream) {
  Plan plan;
  for (int b = 0; b < NWG; ++b) {
    const int h = b >> 3, s = b & 7, e = (s - h - 1) & 7;
    const bool big = h >= NF, slow = !big && (h & 7) == 3;
    const int kp = big ? 512 : slow ? KP2 : KP1, kn = big ? 512 : slow ? KN2 : KN1, k0 = e * kp + (e < 2 ? e : 2) * (kn - kp), nk = e == 7 ? WIN - k0 : e < 2 ? kn : kp;
    const int qi = big ? 96 + s * 8 + h - NF : e < 2 ? 2 * h + e : 46 + (slow ? 21 + (h >> 3) : h - ((h + 4) >> 3)) * 5 + e;
    const bool qjob = (big || e != 7) && qi < 160;
    const int q2 = qi - 96, q3 = qi < 32 ? qi : (qi - 32) >> 1;
    const int qline = q2 >= 0 ? q2 + q2 / 7 + (q2 % 7 >= 3) : q3 < 9 ? q3 * 8 + 3 : 64 + q3, qpart = q2 >= 0 ? !big : qi < 32 ? 2 : qi & 1;
    const int nl = big ? NB : q2 >= 0 ? 20 - NB : qpart == 2 ? 8 : 6, row0 = (q2 >= 0 ? 32 * NB : 192) * qpart;
    plan.w[3 * b] = (unsigned)(h * SK + (SK - WIN) + k0) * (DH * 2);
    plan.w[3 * b + 1] = qjob ? (unsigned)(row0 * 12288 + qline * 128) : OOB;
    plan.w[3 * b + 2] = (unsigned)(nk | (qjob ? nl << 10 | qpart << 14 : 0) | job_of(h, s) << 16);
  }
  plan.w[3 * NWG] = plan.w[3 * NWG + 1] = 0u;
  hipLaunchKernelGGL(k_fused, dim3(NWG), dim3(NTHR), 0, stream, (const unsigned short*)d_in[0], (const unsigned short*)d_in[1],
                     (const unsigned short*)d_in[2], (const unsigned short*)d_in[3], (const unsigned short*)d_in[4],
                     (const unsigned short*)d_in[6], (const unsigned short*)d_in[7], (const unsigned short*)d_in[8],
                     (unsigned short*)d_out, (unsigned char*)d_ws, plan);
}
```

```cpp
#include <hip/hip_runtime.h>
#include <cstdint>

namespace {

constexpr int D = 640, DH = 192, SK = 32768, WIN = 4096, FF = 1536;
constexpr int NWG = 256, NSW = 8, NTHR = 768;
constexpr int KP1 = 544;
constexpr int KN1 = 576;
constexpr int KP2 = 560;
constexpr int KN2 = 576;
constexpr int NB = 12;
constexpr int KW = 6;
constexpr int VW = 6;
constexpr int VN = 4;
constexpr int VP = 6;
constexpr int WS = 3;
constexpr int WS2 = 2;
constexpr int D2 = 30;
constexpr int HP = 48;
constexpr int WW = 4;
constexpr int W2 = 5;
constexpr int DW = 8;
constexpr int SA = 80;
constexpr int SP = 1490;
constexpr int SE = 1570;
constexpr int SW = 1650;
constexpr int TW = 20;
constexpr int PT = 580;
constexpr int QT = 630;
constexpr int G3 = 1810;
constexpr int GR = 1880;
constexpr int QS = 10;
constexpr int MS = 4;
constexpr int OS = 2;
constexpr int OL = 8;
constexpr int OK = 32;
constexpr int PS = 2;
constexpr int PLS = 4;
constexpr int PK = 184;
constexpr int HS = 1;
constexpr int RS = 1;
constexpr int CPF = 16;
static_assert(KW == 6 && WIN - 2 * KN1 - 5 * KP1 >= 128 && WIN - 2 * KN2 - 5 * KP2 >= 128, "the first two key groups are never cut");

constexpr int NF = 24, NP = 120, NR = 80;
constexpr unsigned WS_EPOCH = 0;
constexpr unsigned TWIN = 8192;
__device__ __forceinline__ unsigned prow(unsigned r) { return (r >> 1) * 16384u + (r & 1) * 4096u; }
__device__ __forceinline__ unsigned orow(unsigned o) { return (o >> 13) * 16384u + (o & 8191u); }
constexpr unsigned WS_PR = 16384;
constexpr unsigned WS_P2 = 32768;
constexpr unsigned WS_P3 = WS_P2 + 6 * 16384;
constexpr unsigned O_ROW = 3 * 16384;
constexpr unsigned WS_O = WS_P3 + 12 * 16384;
constexpr unsigned WS_ML = WS_O + 8 * O_ROW;
constexpr unsigned WS_Q = WS_ML + 256 * 128;
constexpr unsigned WS_END = WS_Q + 96 * 1024;
constexpr unsigned OOB = 0x7F000000u;
constexpr int SPIN_CAP = 1 << 17;

typedef unsigned u32x2 __attribute__((ext_vector_type(2)));
typedef unsigned u32x4 __attribute__((ext_vector_type(4)));
typedef unsigned u32x8 __attribute__((ext_vector_type(8)));
typedef float f32x2 __attribute__((ext_vector_type(2)));
typedef float f32x4 __attribute__((ext_vector_type(4)));
typedef __bf16 bf16x2 __attribute__((ext_vector_type(2)));
typedef __bf16 bf16x8 __attribute__((ext_vector_type(8)));
typedef __bf16 bf16x16 __attribute__((ext_vector_type(16)));
typedef __amdgpu_buffer_rsrc_t rsrc_t;
constexpr int AUX_NT = 2, AUX_SC1 = 16;

template <class P> __device__ __forceinline__ rsrc_t mk(P p, unsigned bytes) { return __builtin_amdgcn_make_buffer_rsrc((void*)p, 0, bytes, 0x00020000); }
template <int AUX> __device__ __forceinline__ u32x4 ld128(rsrc_t r, unsigned vo, unsigned so) { return __builtin_amdgcn_raw_buffer_load_b128(r, vo, so, AUX); }
template <int AUX> __device__ __forceinline__ u32x2 ld64(rsrc_t r, unsigned vo, unsigned so) { return __builtin_amdgcn_raw_buffer_load_b64(r, vo, so, AUX); }
template <int AUX> __device__ __forceinline__ unsigned ld32(rsrc_t r, unsigned vo, unsigned so) { return __builtin_amdgcn_raw_buffer_load_b32(r, vo, so, AUX); }
__device__ __forceinline__ void st128(u32x4 v, rsrc_t r, unsigned vo) { __builtin_amdgcn_raw_buffer_store_b128(v, r, vo, 0, AUX_SC1); }
__device__ __forceinline__ void st64(u32x2 v, rsrc_t r, unsigned vo) { __builtin_amdgcn_raw_buffer_store_b64(v, r, vo, 0, AUX_SC1); }

__device__ __forceinline__ float asf(unsigned u) { return __uint_as_float(u); }
__device__ __forceinline__ unsigned asu(float f) { return __float_as_uint(f); }
__device__ __forceinline__ f32x4 asf4(u32x4 v) { return __builtin_bit_cast(f32x4, v); }
__device__ __forceinline__ float lo16(unsigned u) { return asf(u << 16); }
__device__ __forceinline__ float hi16(unsigned u) { return asf(u & 0xFFFF0000u); }
__device__ __forceinline__ unsigned pk(float lo, float hi) { bf16x2 v = {(__bf16)lo, (__bf16)hi}; return __builtin_bit_cast(unsigned, v); }
__device__ __forceinline__ float bfr(float f) { return (float)(__bf16)f; }
__device__ __forceinline__ float dot2(unsigned a, unsigned b, float acc) {
  return __builtin_amdgcn_fdot2_f32_bf16(__builtin_bit_cast(bf16x2, a), __builtin_bit_cast(bf16x2, b), acc, false);
}
__device__ __forceinline__ f32x4 smf4(u32x4 a, u32x8 b, f32x4 c, int idx) {
  return __builtin_amdgcn_smfmac_f32_16x16x64_bf16(__builtin_bit_cast(bf16x8, a), __builtin_bit_cast(bf16x16, b), c, idx, 0, 0);
}
__device__ __forceinline__ float smf(u32x4 a, u32x8 b, int idx) { return smf4(a, b, f32x4{0.f, 0.f, 0.f, 0.f}, idx)[0]; }
__device__ __forceinline__ float mfma(u32x4 a, u32x4 b) {
  return __builtin_amdgcn_mfma_f32_16x16x32_bf16(__builtin_bit_cast(bf16x8, a), __builtin_bit_cast(bf16x8, b), f32x4{0.f, 0.f, 0.f, 0.f}, 0, 0, 0)[0];
}
template <int N> __device__ __forceinline__ void zero(f32x4 (&d)[N]) {
#pragma unroll
  for (int i = 0; i < N; ++i) { d[i] = f32x4{0.f, 0.f, 0.f, 0.f}; asm volatile("" : "+v"(d[i])); }
}
__device__ __forceinline__ int smf_idx(int odd) { return odd ? 0xEEEE : 0x4444; }
constexpr int hrow(int i) { return 4 * (i & 15) + 2 * (i >> 5) + ((i >> 4) & 1); }
constexpr int krow(int j) { return 32 * (j >> 3) + 8 * ((j >> 1) & 1) + 2 * ((j >> 2) & 1) + (j & 1); }
__device__ __forceinline__ int klane(int lane) { return 16 * (lane >> 5) + 4 * ((lane >> 4) & 1); }
__device__ __forceinline__ unsigned plo(unsigned a, unsigned b) { return __builtin_amdgcn_perm(b, a, 0x05040100u); }
__device__ __forceinline__ unsigned phi(unsigned a, unsigned b) { return __builtin_amdgcn_perm(b, a, 0x07060302u); }
template <int CTRL> __device__ __forceinline__ float dpp(float x) {
  return __builtin_bit_cast(float, __builtin_amdgcn_mov_dpp(__builtin_bit_cast(int, x), CTRL, 0xf, 0xf, true));
}
template <int N> __device__ __forceinline__ unsigned shl(unsigned x) { return (unsigned)__builtin_amdgcn_mov_dpp((int)x, 0x100 + N, 0xf, 0xf, true); }
constexpr int XOR1 = 0xB1, XOR2 = 0x4E, HMIR = 0x141, ROR8 = 0x128, SHL4 = 0x104;
__device__ __forceinline__ float tr32(float a, float b) { auto s = __builtin_amdgcn_permlane32_swap(asu(a), asu(b), false, false); return asf(s[0]) + asf(s[1]); }
__device__ __forceinline__ float tr16(float a, float b) { auto s = __builtin_amdgcn_permlane16_swap(asu(a), asu(b), false, false); return asf(s[0]) + asf(s[1]); }
__device__ __forceinline__ float tr8(float a, float b, bool b3) { const float t1 = a + dpp<ROR8>(a), t2 = b + dpp<ROR8>(b); return b3 ? t2 : t1; }
__device__ __forceinline__ float sum8(float x) { x += dpp<XOR1>(x); x += dpp<XOR2>(x); x += dpp<HMIR>(x); return x; }
__device__ __forceinline__ float max8(float x) { x = fmaxf(x, dpp<XOR1>(x)); x = fmaxf(x, dpp<XOR2>(x)); x = fmaxf(x, dpp<HMIR>(x)); return x; }
__device__ __forceinline__ float sumg(float x) { x += dpp<ROR8>(x); x = tr16(x, x); return tr32(x, x); }
__device__ __forceinline__ float maxg(float x) {
  x = fmaxf(x, dpp<ROR8>(x));
  auto s = __builtin_amdgcn_permlane16_swap(asu(x), asu(x), false, false); x = fmaxf(asf(s[0]), asf(s[1]));
  auto t = __builtin_amdgcn_permlane32_swap(asu(x), asu(x), false, false); return fmaxf(asf(t[0]), asf(t[1]));
}
__device__ __forceinline__ float sum64(float x) { return sumg(sum8(x)); }
__device__ __forceinline__ float sum21(float x) { x += dpp<SHL4>(x); x += dpp<XOR2>(x); return x; }
__device__ __forceinline__ float rdl(float v, int l) { return asf((unsigned)__builtin_amdgcn_readlane((int)asu(v), l)); }
__device__ __forceinline__ float rstd(float var) { return __builtin_amdgcn_rcpf(bfr(__builtin_amdgcn_sqrtf(bfr(var + 1e-5f)))); }
__device__ __forceinline__ void bar() {
  __builtin_amdgcn_sched_barrier(0);
  asm volatile("s_waitcnt lgkmcnt(0)\n\ts_barrier" ::: "memory");
  __builtin_amdgcn_sched_barrier(0);
}
#define SCHED() __builtin_amdgcn_sched_barrier(0)
#define LEAVE() asm volatile("s_endpgm" ::: "memory")
#define POLL_LOOP _Pragma("nounroll") for (int spin = 0; spin < SPIN_CAP; ++spin)
#define VMWAIT(n) asm volatile("s_waitcnt vmcnt(%0)" ::"n"(n) : "memory")
#define PACE(W, STEP) asm volatile("s_waitcnt vmcnt(%2) lgkmcnt(0)\n\ts_lshl_b32 s94, s92, 4\n\ts_sub_u32 s94, s94, %0\n\ts_add_u32 %0, %0, %1\n\ts_cmp_lt_i32 s94, 0\n\ts_cbranch_scc0 1f\n\t" \
    "s_waitcnt vmcnt(%3)\n\ts_cmp_lt_i32 s94, %5\n\ts_cbranch_scc0 1f\n\ts_waitcnt vmcnt(%4)\n1:\n\ts_memrealtime s[92:93]" \
    : "+s"(due) : "s"(STEP), "n"((W) - 1), "n"(WS - 1), "n"(WS2 - 1), "n"(-16 * D2) : "s92", "s93", "s94", "scc", "memory")
#define HARD(W, STEP) asm volatile("s_waitcnt vmcnt(%2)\n\ts_add_u32 %0, %0, %1\n1:\n\ts_waitcnt lgkmcnt(0)\n\ts_lshl_b32 s94, s92, 4\n\ts_memrealtime s[92:93]\n\ts_sub_u32 s94, s94, %0\n\t" \
    "s_cmp_lt_i32 s94, 0\n\ts_cbranch_scc1 1b" : "+s"(due) : "s"(STEP), "n"((W) - 1) : "s92", "s93", "s94", "scc", "memory")
#define GATE(W, STEP, I) do { if ((I) >= HP) HARD(W, STEP); else PACE(W, STEP); } while (0)
constexpr float L2E = 1.4426950408889634f;

__device__ __forceinline__ u32x4 make_tag(u32x2 e1) {
  const unsigned a0 = e1[0] & 0x3FFu;
  return u32x4{a0, ((e1[0] >> 10) + a0) & 0x3FFu, ((e1[0] >> 20) + a0) & 0x3FFu, (((e1[0] >> 30) | (e1[1] << 2)) + a0) & 0x3FFu};
}
__device__ __forceinline__ u32x4 tagged(f32x4 o, u32x4 T) {
  u32x4 r;
#pragma unroll
  for (int k = 0; k < 4; ++k) r[k] = __builtin_amdgcn_bitop3_b32(asu(o[k]), ~0x3FFu, T[k], 0xEA);
  return r;
}
template <int N, int S, int MODE, int K> __device__ __forceinline__ void poll(rsrc_t r, const unsigned (&off)[N], u32x4 T, u32x4 (&v)[N]);
template <> __device__ __forceinline__ void poll<1, 1, 0, 0>(rsrc_t r, const unsigned (&off)[1], u32x4 T, u32x4 (&v)[1]) {
  asm volatile(
      "s_mov_b64 s[88:89], exec\ns_mov_b32 s90, 0x20000\nv_cmp_ne_u32 vcc, 0x7f000000, %1\ns_mov_b64 s[80:81], vcc\n.Lp_%=:\ns_mov_b64 exec, s[80:81]\n"
      "s_cbranch_execz .Ls0_%=\nbuffer_load_dwordx4 v[160:163], %1, %6, 0 offen sc1\n.Ls0_%=:\ns_mov_b64 exec, s[88:89]\ns_waitcnt vmcnt(0)\nv_xor_b32 v164, v161, %3\n"
      "v_bitop3_b32 v164, v162, %4, v164 bitop3:0xbe\nv_bitop3_b32 v164, v163, %5, v164 bitop3:0xbe\nv_bitop3_b32 v164, v160, %2, v164 bitop3:0xbe\n"
      "v_and_b32 v164, 0x3ff, v164\nv_cmp_ne_u32 vcc, 0, v164\ns_and_b64 s[80:81], s[80:81], vcc\ns_cbranch_scc0 .Le_%=\ns_sleep 1\ns_sub_u32 s90, s90, 1\n"
      "s_cbranch_scc0 .Lp_%=\n.Le_%=:\n"
      : "+&{v[160:163]}"(v[0])
      : "v"(off[0]), "v"(T[0]), "v"(T[1]), "v"(T[2]), "v"(T[3]), "s"(r)
      : "memory", "vcc", "scc", "v164", "s80", "s81", "s88", "s89", "s90");
}
template <> __device__ __forceinline__ void poll<1, 4, 0, 0>(rsrc_t r, const unsigned (&off)[1], u32x4 T, u32x4 (&v)[1]) {
  asm volatile(
      "s_mov_b64 s[88:89], exec\ns_mov_b32 s90, 0x20000\nv_cmp_ne_u32 vcc, 0x7f000000, %1\ns_mov_b64 s[80:81], vcc\n.Lp_%=:\ns_mov_b64 exec, s[80:81]\n"
      "s_cbranch_execz .Ls0_%=\nbuffer_load_dwordx4 v[160:163], %1, %6, 0 offen sc1\n.Ls0_%=:\ns_mov_b64 exec, s[88:89]\ns_waitcnt vmcnt(0)\nv_xor_b32 v164, v161, %3\n"
      "v_bitop3_b32 v164, v162, %4, v164 bitop3:0xbe\nv_bitop3_b32 v164, v163, %5, v164 bitop3:0xbe\nv_bitop3_b32 v164, v160, %2, v164 bitop3:0xbe\n"
      "v_and_b32 v164, 0x3ff, v164\nv_cmp_ne_u32 vcc, 0, v164\ns_and_b64 s[80:81], s[80:81], vcc\ns_cbranch_scc0 .Le_%=\ns_sleep 4\ns_sub_u32 s90, s90, 1\n"
      "s_cbranch_scc0 .Lp_%=\n.Le_%=:\n"
      : "+&{v[160:163]}"(v[0])
      : "v"(off[0]), "v"(T[0]), "v"(T[1]), "v"(T[2]), "v"(T[3]), "s"(r)
      : "memory", "vcc", "scc", "v164", "s80", "s81", "s88", "s89", "s90");
}
template <> __device__ __forceinline__ void poll<2, 2099202, 5, 0>(rsrc_t r, const unsigned (&off)[2], u32x4 T, u32x4 (&v)[2]) {
  asm volatile(
      "s_mov_b64 s[88:89], exec\ns_mov_b32 s90, 0x20000\nv_cmp_ne_u32 vcc, 0x7f000000, %2\ns_mov_b64 s[80:81], vcc\nv_cmp_ne_u32 vcc, 0x7f000000, %3\n"
      "s_mov_b64 s[82:83], vcc\ns_branch .Lc_%=\n.Lp_%=:\ns_mov_b64 exec, s[80:81]\ns_cbranch_execz .Ls0_%=\nbuffer_load_dwordx4 v[156:159], %2, %8, 0 offen sc1\n"
      ".Ls0_%=:\ns_mov_b64 exec, s[82:83]\ns_cbranch_execz .Ls1_%=\nbuffer_load_dwordx4 v[160:163], %3, %8, 0 offen sc1\n.Ls1_%=:\ns_mov_b64 exec, s[88:89]\n"
      "s_waitcnt vmcnt(0)\n.Lc_%=:\nv_xor_b32 v164, v157, %5\nv_bitop3_b32 v164, v158, %6, v164 bitop3:0xbe\nv_bitop3_b32 v164, v159, %7, v164 bitop3:0xbe\n"
      "v_bitop3_b32 v164, v156, %4, v164 bitop3:0xbe\nv_and_b32 v164, 0x3ff, v164\nv_cmp_ne_u32 vcc, 0, v164\ns_and_b64 s[80:81], s[80:81], vcc\n"
      "v_xor_b32 v164, v161, %5\nv_bitop3_b32 v164, v162, %6, v164 bitop3:0xbe\nv_bitop3_b32 v164, v163, %7, v164 bitop3:0xbe\n"
      "v_bitop3_b32 v164, v160, %4, v164 bitop3:0xbe\nv_and_b32 v164, 0x3ff, v164\nv_cmp_ne_u32 vcc, 0, v164\ns_and_b64 s[82:83], s[82:83], vcc\n"
      "s_or_b64 s[92:93], s[80:81], s[82:83]\ns_cbranch_scc0 .Le_%=\ns_bcnt1_i32_b64 s96, s[80:81]\ns_mov_b32 s91, s96\ns_bcnt1_i32_b64 s96, s[82:83]\n"
      "s_add_u32 s91, s91, s96\ns_cmp_le_u32 s91, 32\ns_cbranch_scc1 .Lq_%=\ns_sleep 8\n.Lq_%=:\ns_sleep 2\ns_sub_u32 s90, s90, 1\ns_cbranch_scc0 .Lp_%=\n.Le_%=:\n"
      : "+&{v[156:159]}"(v[0]), "+&{v[160:163]}"(v[1])
      : "v"(off[0]), "v"(off[1]), "v"(T[0]), "v"(T[1]), "v"(T[2]), "v"(T[3]), "s"(r)
      : "memory", "vcc", "scc", "v164", "s80", "s81", "s82", "s83", "s88", "s89", "s90", "s92", "s93", "s91", "s96");
}
template <> __device__ __forceinline__ void poll<3, 10, 0, 0>(rsrc_t r, const unsigned (&off)[3], u32x4 T, u32x4 (&v)[3]) {
  asm volatile(
      "s_mov_b64 s[88:89], exec\ns_mov_b32 s90, 0x20000\nv_cmp_ne_u32 vcc, 0x7f000000, %3\ns_mov_b64 s[80:81], vcc\nv_cmp_ne_u32 vcc, 0x7f000000, %4\n"
      "s_mov_b64 s[82:83], vcc\nv_cmp_ne_u32 vcc, 0x7f000000, %5\ns_mov_b64 s[84:85], vcc\n.Lp_%=:\ns_mov_b64 exec, s[80:81]\ns_cbranch_execz .Ls0_%=\n"
      "buffer_load_dwordx4 v[152:155], %3, %10, 0 offen sc1\n.Ls0_%=:\ns_mov_b64 exec, s[82:83]\ns_cbranch_execz .Ls1_%=\n"
      "buffer_load_dwordx4 v[156:159], %4, %10, 0 offen sc1\n.Ls1_%=:\ns_mov_b64 exec, s[84:85]\ns_cbranch_execz .Ls2_%=\n"
      "buffer_load_dwordx4 v[160:163], %5, %10, 0 offen sc1\n.Ls2_%=:\ns_mov_b64 exec, s[88:89]\ns_waitcnt vmcnt(0)\nv_xor_b32 v164, v153, %7\n"
      "v_bitop3_b32 v164, v154, %8, v164 bitop3:0xbe\nv_bitop3_b32 v164, v155, %9, v164 bitop3:0xbe\nv_bitop3_b32 v164, v152, %6, v164 bitop3:0xbe\n"
      "v_and_b32 v164, 0x3ff, v164\nv_cmp_ne_u32 vcc, 0, v164\ns_and_b64 s[80:81], s[80:81], vcc\nv_xor_b32 v164, v157, %7\n"
      "v_bitop3_b32 v164, v158, %8, v164 bitop3:0xbe\nv_bitop3_b32 v164, v159, %9, v164 bitop3:0xbe\nv_bitop3_b32 v164, v156, %6, v164 bitop3:0xbe\n"
      "v_and_b32 v164, 0x3ff, v164\nv_cmp_ne_u32 vcc, 0, v164\ns_and_b64 s[82:83], s[82:83], vcc\ns_or_b64 s[92:93], s[80:81], s[82:83]\nv_xor_b32 v164, v161, %7\n"
      "v_bitop3_b32 v164, v162, %8, v164 bitop3:0xbe\nv_bitop3_b32 v164, v163, %9, v164 bitop3:0xbe\nv_bitop3_b32 v164, v160, %6, v164 bitop3:0xbe\n"
      "v_and_b32 v164, 0x3ff, v164\nv_cmp_ne_u32 vcc, 0, v164\ns_and_b64 s[84:85], s[84:85], vcc\ns_or_b64 s[92:93], s[92:93], s[84:85]\ns_cbranch_scc0 .Le_%=\n"
      "s_sleep 10\ns_sub_u32 s90, s90, 1\ns_cbranch_scc0 .Lp_%=\n.Le_%=:\n"
      : "+&{v[152:155]}"(v[0]), "+&{v[156:159]}"(v[1]), "+&{v[160:163]}"(v[2])
      : "v"(off[0]), "v"(off[1]), "v"(off[2]), "v"(T[0]), "v"(T[1]), "v"(T[2]), "v"(T[3]), "s"(r)
      : "memory", "vcc", "scc", "v164", "s80", "s81", "s82", "s83", "s84", "s85", "s88", "s89", "s90", "s92", "s93");
}
template <> __device__ __forceinline__ void poll<3, 12059650, 0, 0>(rsrc_t r, const unsigned (&off)[3], u32x4 T, u32x4 (&v)[3]) {
  asm volatile(
      "s_mov_b64 s[88:89], exec\ns_mov_b32 s90, 0x20000\nv_cmp_ne_u32 vcc, 0x7f000000, %3\ns_mov_b64 s[80:81], vcc\nv_cmp_ne_u32 vcc, 0x7f000000, %4\n"
      "s_mov_b64 s[82:83], vcc\nv_cmp_ne_u32 vcc, 0x7f000000, %5\ns_mov_b64 s[84:85], vcc\n.Lp_%=:\ns_mov_b64 exec, s[80:81]\ns_cbranch_execz .Ls0_%=\n"
      "buffer_load_dwordx4 v[152:155], %3, %10, 0 offen sc1\n.Ls0_%=:\ns_mov_b64 exec, s[82:83]\ns_cbranch_execz .Ls1_%=\n"
      "buffer_load_dwordx4 v[156:159], %4, %10, 0 offen sc1\n.Ls1_%=:\ns_mov_b64 exec, s[84:85]\ns_cbranch_execz .Ls2_%=\n"
      "buffer_load_dwordx4 v[160:163], %5, %10, 0 offen sc1\n.Ls2_%=:\ns_mov_b64 exec, s[88:89]\ns_waitcnt vmcnt(0)\nv_xor_b32 v164, v153, %7\n"
      "v_bitop3_b32 v164, v154, %8, v164 bitop3:0xbe\nv_bitop3_b32 v164, v155, %9, v164 bitop3:0xbe\nv_bitop3_b32 v164, v152, %6, v164 bitop3:0xbe\n"
      "v_and_b32 v164, 0x3ff, v164\nv_cmp_ne_u32 vcc, 0, v164\ns_and_b64 s[80:81], s[80:81], vcc\nv_xor_b32 v164, v157, %7\n"
      "v_bitop3_b32 v164, v158, %8, v164 bitop3:0xbe\nv_bitop3_b32 v164, v159, %9, v164 bitop3:0xbe\nv_bitop3_b32 v164, v156, %6, v164 bitop3:0xbe\n"
      "v_and_b32 v164, 0x3ff, v164\nv_cmp_ne_u32 vcc, 0, v164\ns_and_b64 s[82:83], s[82:83], vcc\ns_or_b64 s[92:93], s[80:81], s[82:83]\nv_xor_b32 v164, v161, %7\n"
      "v_bitop3_b32 v164, v162, %8, v164 bitop3:0xbe\nv_bitop3_b32 v164, v163, %9, v164 bitop3:0xbe\nv_bitop3_b32 v164, v160, %6, v164 bitop3:0xbe\n"
      "v_and_b32 v164, 0x3ff, v164\nv_cmp_ne_u32 vcc, 0, v164\ns_and_b64 s[84:85], s[84:85], vcc\ns_or_b64 s[92:93], s[92:93], s[84:85]\ns_cbranch_scc0 .Le_%=\n"
      "s_bcnt1_i32_b64 s96, s[80:81]\ns_mov_b32 s91, s96\ns_bcnt1_i32_b64 s96, s[82:83]\ns_add_u32 s91, s91, s96\ns_bcnt1_i32_b64 s96, s[84:85]\ns_add_u32 s91, s91, s96\n"
      "s_cmp_le_u32 s91, 184\ns_cbranch_scc1 .Lq_%=\ns_sleep 4\n.Lq_%=:\ns_sleep 2\ns_sub_u32 s90, s90, 1\ns_cbranch_scc0 .Lp_%=\n.Le_%=:\n"
      : "+&{v[152:155]}"(v[0]), "+&{v[156:159]}"(v[1]), "+&{v[160:163]}"(v[2])
      : "v"(off[0]), "v"(off[1]), "v"(off[2]), "v"(T[0]), "v"(T[1]), "v"(T[2]), "v"(T[3]), "s"(r)
      : "memory", "vcc", "scc", "v164", "s80", "s81", "s82", "s83", "s84", "s85", "s88", "s89", "s90", "s92", "s93", "s91", "s96");
}
template <int STRIDE, int S, int MODE = 0, int K = 0, int N> __device__ __forceinline__ void gather(rsrc_t rws, unsigned off0, u32x4 T, f32x4 (&o)[N]) {
  unsigned off[N];
  u32x4 v[N];
#pragma unroll
  for (int j = 0; j < N; ++j) { off[j] = off0 >= OOB ? OOB : off0 + j * STRIDE; v[j] = T; }
  SCHED();
  poll<N, S, MODE, K>(rws, off, T, v);
  SCHED();
#pragma unroll
  for (int j = 0; j < N; ++j) o[j] = asf4(v[j] ^ T);
}
__device__ __forceinline__ void turn(const u32x4* w, u32x8* o) {
#pragma unroll
  for (int k = 0; k < 4; ++k)
#pragma unroll
    for (int r = 0; r < 8; ++r) { o[2 * k][r] = plo(w[2 * r][k], w[2 * r + 1][k]); o[2 * k + 1][r] = phi(w[2 * r][k], w[2 * r + 1][k]); }
#pragma unroll
  for (int i = 0; i < 8; ++i) asm volatile("" : "+v"(o[i]));
}
constexpr int krow2(int t) { return 32 * (t >> 2) + (t & 3); }
__device__ __forceinline__ void turn2(const u32x4* w, u32x8* o) {
#pragma unroll
  for (int k = 0; k < 4; ++k)
#pragma unroll
    for (int r = 0; r < 4; ++r) { o[k][2 * r] = plo(w[2 * r][k], w[2 * r + 1][k]); o[k][2 * r + 1] = phi(w[2 * r][k], w[2 * r + 1][k]); }
#pragma unroll
  for (int i = 0; i < 4; ++i) asm volatile("" : "+v"(o[i]));
}
template <int S = 2> __device__ __forceinline__ void lds_wait(unsigned* w, unsigned want) {
  POLL_LOOP {
    if (__hip_atomic_load(w, __ATOMIC_RELAXED, __HIP_MEMORY_SCOPE_WORKGROUP) >= want) break;
    if (S) __builtin_amdgcn_s_sleep(S);
  }
  asm volatile("" ::: "memory");
}
__device__ __forceinline__ void lds_post(unsigned* w, int lane, unsigned n = 1u) {
  asm volatile("s_waitcnt lgkmcnt(0)" ::: "memory");
  if (lane == 0) __hip_atomic_fetch_add(w, n, __ATOMIC_RELAXED, __HIP_MEMORY_SCOPE_WORKGROUP);
}

__device__ __forceinline__ void raise(unsigned* w) { asm volatile("" ::: "memory"); __hip_atomic_store(w, 1u, __ATOMIC_RELAXED, __HIP_MEMORY_SCOPE_WORKGROUP); }
template <int S> __device__ __forceinline__ void await(unsigned* w, int n) {
  POLL_LOOP {
    if (__builtin_popcountll(__builtin_amdgcn_ballot_w64(__hip_atomic_load(w, __ATOMIC_RELAXED, __HIP_MEMORY_SCOPE_WORKGROUP) != 0u)) >= n) break;
    if (S) __builtin_amdgcn_s_sleep(S);
  }
  asm volatile("" ::: "memory");
}
__device__ __forceinline__ float score(const u32x4* k, const u32x4* q) {
  float d0 = 0.f, d1 = 0.f;
#pragma unroll
  for (int j = 0; j < 3; ++j) {
    d0 = dot2(k[j][0], q[j][0], d0); d1 = dot2(k[j][1], q[j][1], d1);
    d0 = dot2(k[j][2], q[j][2], d0); d1 = dot2(k[j][3], q[j][3], d1);
  }
  return bfr(bfr(sum8(d0 + d1)) * 0.07216878364870322f);
}
__device__ __forceinline__ void pv8(u32x4 v, unsigned pl, float* acc) {
  const unsigned ph = pl << 16;
#pragma unroll
  for (int k = 0; k < 4; ++k) { acc[2 * k] = dot2(v[k], pl, acc[2 * k]); acc[2 * k + 1] = dot2(v[k], ph, acc[2 * k + 1]); }
}
__device__ __forceinline__ float fold8(const float* acc, int lane) {
  return tr8(tr16(tr32(acc[0], acc[4]), tr32(acc[2], acc[6])), tr16(tr32(acc[1], acc[5]), tr32(acc[3], acc[7])), lane & 8);
}

struct __attribute__((aligned(16))) Lds {
  float ored[NSW][DH];
  float pred[NSW + 1][64];
  float fred[10][128];
  float cred[10][128];
  unsigned short qs[DH];
  unsigned short act[72];
  float ml[NSW][2];
  unsigned tag[4];
  unsigned cnt[4];
  unsigned flag[24];
  unsigned hsum[2][32];
  unsigned near;
  unsigned pad[1];
  u32x4 wst[2][512][8];
};

struct Plan { unsigned w[3 * NWG + 2]; };
int job_of(int h, int s) {
  const int e = (s - h - 1) & 7;
  return h >= NF ? h * 8 + s : e == 7 ? h : e >= 2 ? NF + s * 15 + (h >> 3) * 5 + e - 2 : NF + NP + 2 * h + e;
}
#define UNDEF8(w) asm volatile("" : "=v"(w[0]), "=v"(w[1]), "=v"(w[2]), "=v"(w[3]), "=v"(w[4]), "=v"(w[5]), "=v"(w[6]), "=v"(w[7]))
#define LOFF(x) (unsigned)((const char*)&(x) - (const char*)&L)
#define LAT(T, off) ((T*)((char*)&L + (off)))
typedef const unsigned short* cptr;
typedef const __attribute__((address_space(1))) unsigned short* gptr;
typedef const __attribute__((address_space(4))) unsigned long long* kargs_t;
#define KARGS kargs_t ka = (kargs_t)__builtin_amdgcn_kernarg_segment_ptr(); asm volatile("" : "+s"(ka))
#define ARG(i) ((gptr)ka[i])
__global__ __launch_bounds__(NTHR, 3) void k_fused(cptr, cptr, cptr, cptr, cptr, cptr, cptr, cptr, unsigned short*, unsigned char*, Plan) {
  __shared__ Lds L;
  asm volatile(".Lcode_begin:\n\ts_mul_i32 vcc_lo, %1, 12\n\ts_load_dwordx4 s[96:99], %0, 0x8\n\ts_load_dwordx4 s[92:95], %0, vcc_lo offset:0x50\n\ts_load_dwordx2 s[100:101], %0, 0x18"
               ::"s"(__builtin_amdgcn_kernarg_segment_ptr()), "s"(blockIdx.x) : "vcc", "s92", "s93", "s94", "s95", "s96", "s97", "s98", "s99", "s100", "s101");
  SCHED();
  const unsigned tid0 = __builtin_amdgcn_readfirstlane(threadIdx.x);
  int wave = tid0 >> 6;
  {
    unsigned t;
    asm volatile("s_or_b32 s88, %1, %2\n\ts_cmp_lt_u32 s88, %3\n\ts_cbranch_scc0 1f\n\ts_getpc_b64 s[88:89]\n\ts_mov_b32 m0, %4\n\tv_lshlrev_b32 %0, 7, %5\n\ts_andn2_b32 s88, s88, 0xff\n\ts_and_b32 s89, s89, 0xffff\n\t"
                 "s_mov_b32 s90, .Lcode_end-.Lcode_begin\n\ts_max_u32 s90, s90, .Lcode_end2-.Lcode_begin\n\ts_max_u32 s90, s90, .Lcode_end3-.Lcode_begin\n\ts_mov_b32 s91, 0x20000\n\t"
                 "s_movk_i32 s87, 0x2000\n\tbuffer_load_dword %0, s[88:91], 0 offen lds\n\tbuffer_load_dword %0, s[88:91], s87 offen lds\n\ts_movk_i32 s87, 0x4000\n\t"
                 "buffer_load_dword %0, s[88:91], s87 offen lds\n1:"
                 : "=&v"(t) : "s"(blockIdx.x), "s"(tid0), "n"(CPF), "n"(0), "v"(threadIdx.x) : "memory", "scc", "s87", "s88", "s89", "s90", "s91");
    SCHED();
  }
  u32x4 first[6];
  gptr kc0, vc0, wq;
  unsigned kvo, plan, vq;
  unsigned long long tk0;
  {
    const bool st = wave < NSW;
    unsigned so[6], idx = (threadIdx.x >> 3) & 63u, c16 = (threadIdx.x & 7u) * 16u, size = st ? 32u * SK * DH * 2u : D * 6144u * 2u;
#pragma unroll
    for (int i = 1; i < 6; ++i) so[i] = st ? (i % 3) * 128 + (i / 3) * 24576 : i * 12288;
    asm volatile("" : "+s"(so[1]), "+s"(so[2]), "+s"(so[3]), "+s"(so[4]), "+s"(so[5]), "+s"(size));
    asm volatile("" : "+v"(idx), "+v"(c16));
    gptr p;
    asm volatile("s_cmp_lt_u32 %3, 8\n\ts_waitcnt lgkmcnt(0)\n\ts_cselect_b32 s93, s92, s93\n\tv_add_u32 %1, s93, %4\n\ts_cselect_b64 %0, s[96:97], s[100:101]\n\t"
                 "s_cselect_b32 s95, 0, s94\n\ts_bfe_u32 s95, s95, 0x4000a\n\ts_mul_i32 s95, s95, 0x3000\n\ts_max_u32 s95, s95, 0x180\n\tv_mad_u32_u24 %1, %2, s95, %1"
                 : "=&s"(p), "=&v"(vq) : "v"(idx), "s"(wave), "v"(c16) : "scc", "s93", "s95");
    const rsrc_t r = mk(p, size);
#pragma unroll
    for (int i = 0; i < 6; ++i) {
      if (i == 0) { SCHED(); tk0 = __builtin_amdgcn_s_memrealtime(); SCHED(); }
      first[i] = ld128<AUX_NT>(r, vq, i ? so[i] : 0u);
    }
    SCHED();
    asm volatile("s_mov_b64 %0, s[96:97]\n\ts_mov_b64 %1, s[98:99]\n\ts_mov_b64 %2, s[100:101]\n\ts_mov_b32 %3, s92\n\ts_mov_b32 %4, s94" : "=s"(kc0), "=s"(vc0), "=s"(wq), "=s"(kvo), "=s"(plan));
  }
  int lane = threadIdx.x & 63, b = blockIdx.x;
  asm volatile("" : "+v"(lane));
  asm volatile("" : "+s"(wave), "+s"(b), "+s"(plan));
  const int h = b >> 3, s = b & 7;
  const int g = lane >> 3, c = lane & 7, tk = plan >> 16;
  const bool stream = wave < NSW, isF = tk < NF;
  const int a = wave - NSW;
  u32x2 e1 = {0u, 0u};
  u32x4 T = {0u, 0u, 0u, 0u};
  u32x4 wt[8];

  if (__builtin_expect(stream, 1)) {
    if (wave < 2) {
      const unsigned t = wave * 64 + lane, i = t - 12u;
      if (t < 94u) L.cnt[t] = i < 16u ? i >= 10u : i < 80u && ((i - 16u) & 31u) < 20u ? ~0u : 0u;
    }
    const int nk = plan & 1023;
    const unsigned voff = vq - kvo;
    const rsrc_t rk = mk(kc0 + kvo / 2, nk * 384), rv = mk(vc0 + kvo / 2, nk * 384);
    u32x4 kb[27], vb[27];
    unsigned due, tau, tau8;
#pragma unroll
    for (int i = 0; i < 27; ++i) {
      if (i == KW) bar();
      if (i == KW) {
        const bool nine = 512 + 8 * wave < nk;
        tau = isF ? 0u : tk < NF + NP ? (nine ? 16 * (SP - SA) / 53 : 16 * (SP - SA) / 47) : nine ? 16 * (SE - SA) / 53 : 16 * (SE - SA) / 47;
        tau8 = nine ? tau : 0u;
        due = ((unsigned)tk0 << 4) + (isF ? -65536 : 16 * SA) + (KW - 1) * tau;
        asm volatile("s_memrealtime s[92:93]" : "+s"(tau), "+s"(tau8) :: "s92", "s93");
      }
      if (i >= KW) GATE(KW, i < 24 ? tau : tau8, i);
      kb[i] = i < 6 ? first[i] : ld128<AUX_NT>(rk, voff + (i % 3) * 128, (i / 3) * 24576);
      SCHED();
    }
#define LISTLOAD(n) vb[n] = ld128<AUX_NT>(rv, voff + ((n) % 3) * 128, (8 - (n) / 3) * 24576)
#pragma unroll
    for (int n = 0; n < VW; ++n) {
      GATE(KW, n < 3 ? tau8 : tau, 27 + n);
      LISTLOAD(n);
      SCHED();
    }
    lds_wait(&L.cnt[1], 1);
    u32x4 qv[3];
#pragma unroll
    for (int j = 0; j < 3; ++j) qv[j] = *(const u32x4*)&L.qs[j * 64 + c * 8];
    float sc[9];
#pragma unroll
    for (int t = 0; t < 9; ++t) {
      sc[t] = score(kb + 3 * t, qv);
      if (t & 1) {
        SCHED();
#pragma unroll
        for (int n = VW + VN * (t - 1) / 8; n < VW + VN * (t + 1) / 8; ++n) LISTLOAD(n);
        SCHED();
      }
    }
    due += VN * tau;
    asm volatile("s_memrealtime s[92:93]" : "+s"(due) :: "s92", "s93");
    constexpr int V1 = VW + VN;
#pragma unroll
    for (int t = 1; t < 9; ++t) if (64 * t + 8 * wave >= nk) sc[t] = -__builtin_inff();
    float m = sc[0];
#pragma unroll
    for (int t = 1; t < 9; ++t) m = fmaxf(m, sc[t]);
    m = maxg(m);
    float lsum = 0.f;
    unsigned pp[9];
#pragma unroll
    for (int t = 0; t < 9; ++t) { const float p = __builtin_amdgcn_exp2f((sc[t] - m) * L2E); lsum += p; pp[t] = pk(p, 0.f); }
    lsum = sumg(lsum);
    if (lane == 0) *(f32x2*)L.ml[wave] = f32x2{m, lsum};
    lds_post(&L.cnt[0], lane);
    float acc[24];
    unsigned odst = LOFF(L.ored[wave][8 * c + g]);
    KARGS;
    const rsrc_t rw = mk(isF ? ARG(7) : ARG(4), isF ? FF * D * 2 : tk < NF + NP ? 6144 * D * 2 : 0);
    unsigned wvo = (unsigned)(isF ? klane(lane) + (lane & 8) : (lane >> 4) + (lane & 8)) * 1280u + c * 16u;
    int ws0 = isF ? 64 * tk * 1280 + wave * 128 : ((tk - NF) / 10 * 512 + 64 * wave) * 1280 + (tk - NF) % 10 * 128, ws1 = isF ? 1280 : 4 * 1280, ws2 = isF ? 2 * 1280 : 16 * 1280;
    const rsrc_t rws = mk(ARG(9), WS_END);
    unsigned oo = WS_O - 1u + (unsigned)g * O_ROW + orow((unsigned)((tk - NF) / 10 * 512 + 64 * wave + 4 * c) * 4u);
    asm volatile("" : "+v"(odst), "+v"(wvo), "+v"(oo));
    asm volatile("" : "+s"(ws0), "+s"(ws1), "+s"(ws2));
#define WLOAD(t) wt[t] = ld128<AUX_NT>(rw, wvo, ws0 + ((t) & 1) * ws1 + (((t) >> 1) & 1) * ws2 + ((t) >> 2) * 32 * 1280)
#pragma unroll
    for (int e = 0; e < 24; ++e) acc[e] = 0.f;
#pragma unroll
    for (int n = 0; n < 27; ++n) {
      if (n + VP >= V1 && n + VP < 27) { GATE(VP, tau, 27 + n + VP); LISTLOAD(n + VP); }
      SCHED();
      pv8(vb[n], pp[8 - n / 3], acc + 8 * (n % 3));
      if (n + WW >= 27 && n < 26) { SCHED(); WLOAD(n + WW - 27); SCHED(); }
      if (n >= 24) *LAT(float, odst + (n % 3) * 256) = fold8(acc + 8 * (n % 3), lane);
      SCHED();
    }
    if (tk >= NF + NP) {
      bar();
      LEAVE();
    } else {
      raise(&L.flag[wave]);
    }
    if (isF) {
#pragma unroll
      for (int t = WW - 1; t < 8; ++t) { VMWAIT(W2 - 1); WLOAD(t); SCHED(); }
    } else if (tk < NF + NP) {
      T = *(const u32x4*)L.tag;
      lds_wait(&L.near, 1);
      oo += L.near;
      unsigned off[2] = {oo, oo + 128u}, pdst = LOFF(L.pred[g == 0 ? wave : NSW][8 * c]);
      int idx = smf_idx(lane & 1);
      asm volatile("" : "+v"(pdst), "+v"(idx), "+v"(off[0]), "+v"(off[1]), "+v"(T));
      f32x4 d[4];
      zero(d);
      SCHED();
#pragma unroll
      for (int t = WW - 1; t < 8; ++t) {
        VMWAIT(W2 - 1);
        while ((int)((unsigned)__builtin_amdgcn_s_memrealtime() - ((unsigned)tk0 + SW - (7 - t) * TW)) < 0) __builtin_amdgcn_s_sleep(1);
        WLOAD(t);
        SCHED();
      }
      VMWAIT(0);
      u32x4 v[2] = {ld128<AUX_SC1>(rws, off[0], 0), ld128<AUX_SC1>(rws, off[1], 0)};
      SCHED();
      u32x8 wp[4];
      turn2(wt, wp);
      SCHED();
      poll<2, OS + 256 * OL + 65536 * OK, 5, 0>(rws, off, T, v);
      SCHED();
      float o8[8];
#pragma unroll
      for (int i = 0; i < 8; ++i) o8[i] = asf(v[i >> 2][i & 3] ^ T[i & 3]);
      const float o1 = fold8(o8, lane);
      const unsigned op = pk(dpp<0xA0>(o1), dpp<0xF5>(o1));
      const u32x4 av = {op, shl<4>(op), shl<8>(op), shl<12>(op)};
#pragma unroll
      for (int k = 0; k < 4; ++k) d[k] = smf4(av, wp[k], d[k], idx);
      SCHED();
#pragma unroll
      for (int k = 0; k < 4; k += 2) {
        f32x4 r = {d[k][0] + dpp<ROR8>(d[k][2]), 0.f, d[k + 1][0] + dpp<ROR8>(d[k + 1][2]), 0.f};
        asm volatile("" : "+v"(r[0]), "+v"(r[2]));
        r[1] = d[k][1] + dpp<ROR8>(d[k][3]); r[3] = d[k + 1][1] + dpp<ROR8>(d[k + 1][3]);
        *LAT(f32x4, pdst + 8 * k) = r;
      }
      bar();
      LEAVE();
    }
  } else if (isF && a < 2) {
    __builtin_amdgcn_s_setprio(3);
    KARGS;
    const rsrc_t rw = mk((a ? ARG(6) : ARG(5)) + 64 * h, D * FF * 2 - 128 * h);
    const unsigned vo = (unsigned)g * 3072u + c * 16u;
    unsigned so = 0u, dst = LOFF(L.wst[a]) + (unsigned)(size_t)&L;
#define DMA() asm volatile("s_mov_b32 m0, %1\n\ts_add_u32 %1, %1, 0x400\n\tbuffer_load_dwordx4 %2, %3, %0 offen nt lds\n\ts_add_u32 %0, %0, 0x6000" : "+s"(so), "+s"(dst) : "v"(vo), "s"(rw) : "memory", "scc")
    bar();
#pragma nounroll
    for (int i = 0; i < 64; ++i) { VMWAIT(DW - 1); DMA(); }
    asm volatile(".Lcode_end3:");
    UNDEF8(wt);
  } else {
    __builtin_amdgcn_s_setprio(3);
    bar();
    const int nl = (plan >> 10) & 15, qpart = (plan >> 14) & 3;
    const bool big = nl > 8, qjob = nl != 0;
    constexpr int NS = 8;
    const rsrc_t rq = mk(wq, D * 6144 * 2);
    u32x4 wb[NB];
#pragma unroll
    for (int i = 0; i < NS; ++i) wb[i] = i < 6 ? first[i] : ld128<AUX_NT>(rq, nl > 6 ? vq : OOB, i * 12288);
    if (big) {
#pragma unroll
      for (int i = NS; i < NB; ++i) wb[i] = ld128<AUX_NT>(rq, vq, i * 12288);
    }
    const unsigned row = vq / 12288u, qline = __builtin_amdgcn_readfirstlane((vq - row * 12288u) >> 7), xo = qjob ? row * 2u : OOB;
    SCHED();
    KARGS;
    const rsrc_t rws = mk(ARG(9), WS_END), rx = mk(ARG(0), D * 2);
    const u32x4 xs4 = ld128<0>(rx, (unsigned)lane * 16u, 0);
    const unsigned xs1 = ld32<0>(rx, 1024u + (unsigned)lane * 4u, 0);
    const u32x4 xr0 = ld128<0>(rx, xo, 0), xr4 = ld128<0>(rx, xo + 16u, 0);
    const u32x2 ep = ld64<AUX_SC1>(rws, WS_EPOCH, 0);
    unsigned pf[3] = {0u, 0u, 0u};
    if (a == 3) {
      pf[0] = ld32<0>(mk(ARG(8), D * 2), 0, 0);
      const unsigned kvn = (plan & 1023u) * 384u, po = lane == 0 ? 0u : lane == 1 ? kvn - 128u : OOB;
      pf[1] = ld32<AUX_NT>(mk(vc0 + kvo / 2, kvn), po, 0);
      pf[2] = ld32<AUX_NT>(mk(kc0 + kvo / 2, kvn), lane == 1 ? po : OOB, 0);
    }
    f32x4 qp = {0.f, 0.f, 0.f, 0.f};
    if (qjob) {
      const unsigned xs[5] = {xs4[0], xs4[1], xs4[2], xs4[3], xs1}, xr[7] = {xr0[0], xr0[1], xr0[2], xr0[3], xr4[0], xr4[1], xr4[2]};
      float sm = 0.f;
#pragma unroll
      for (int i = 0; i < 5; ++i) sm += lo16(xs[i]) + hi16(xs[i]);
      const float mean = sum64(sm) * (1.f / D);
      float sq = 0.f;
#pragma unroll
      for (int i = 0; i < 5; ++i) { const float d0 = lo16(xs[i]) - mean, d1 = hi16(xs[i]) - mean; sq += d0 * d0 + d1 * d1; }
      const float mb = bfr(mean), rden = rstd(bfr(sum64(sq) * (1.f / D)));
      float qa[8];
#pragma unroll
      for (int k = 0; k < 8; ++k) qa[k] = 0.f;
      auto rows = [&](int i) {
        const unsigned xp = pk(bfr(lo16(xr[i]) - mb) * rden, bfr(hi16(xr[i]) - mb) * rden);
#pragma unroll
        for (int k = 0; k < 4; ++k) {
          qa[2 * k] = dot2(plo(wb[2 * i][k], wb[2 * i + 1][k]), xp, qa[2 * k]);
          qa[2 * k + 1] = dot2(phi(wb[2 * i][k], wb[2 * i + 1][k]), xp, qa[2 * k + 1]);
        }
      };
#pragma unroll
      for (int i = 0; i < NS / 2; ++i) rows(i);
      if (big) {
#pragma unroll
        for (int i = NS / 2; i < NB / 2; ++i) rows(i);
      }
#pragma unroll
      for (int i = 0; i < 4; ++i) { float t = tr32(qa[i], qa[i + 4]); t = tr16(t, t); qp[i] = t + dpp<ROR8>(t); }
    }
    if ((lane & 24) == 0) *(f32x4*)&L.pred[a][c * 8 + (lane >> 5) * 4] = qp;
    lds_post(&L.cnt[3], lane);
    const unsigned long long en = (((unsigned long long)ep[1] << 32) | ep[0]) + 1ull;
    e1 = u32x2{(unsigned)en, (unsigned)(en >> 32)};
    T = make_tag(e1);
    if (a == 0 && qjob) {
      lds_wait(&L.cnt[3], 4);
      const float* src = &L.pred[0][4 * (lane & 15)];
      qp = (*(const f32x4*)src + *(const f32x4*)(src + 64)) + (*(const f32x4*)(src + 128) + *(const f32x4*)(src + 192));
      const unsigned ex = asu(fmaxf(fmaxf(fabsf(qp[0]), fabsf(qp[1])), fmaxf(fabsf(qp[2]), fabsf(qp[3])))) >> 23;
      u32x4 qo;
#pragma unroll
      for (int i = 0; i < 4; ++i) qo[i] = ((unsigned)min((int)__builtin_rintf(__builtin_ldexpf(qp[i], 145 - (int)ex)), 524287) << 12) | (((ex >> (2 * i)) & 3u) << 10) | T[i];
      st128(qo, rws, lane < 16 ? WS_Q + (unsigned)qline * 1024u + qpart * 256u + lane * 16u : OOB);
    } else if (a == 2) {
      unsigned off[3];
      u32x4 v[3];
#pragma unroll
      for (int j = 0; j < 3; ++j) {
        const int ql = 3 * h + j;
        off[j] = lane < (ql >= 73 || (ql & 7) == 3 ? 48 : 32) ? WS_Q + (unsigned)ql * 1024u + lane * 16u : OOB;
        v[j] = T;
      }
      while (__builtin_amdgcn_s_memrealtime() < tk0 + QT) __builtin_amdgcn_s_sleep(4);
      SCHED();
      poll<3, QS, 0, 0>(rws, off, T, v);
      SCHED();
#pragma unroll
      for (int j = 0; j < 3; ++j) {
        const int ex = (int)(((v[j][0] >> 10) & 3u) | ((v[j][1] >> 8) & 12u) | ((v[j][2] >> 6) & 48u) | ((v[j][3] >> 4) & 192u)) - 145;
        f32x4 qf;
#pragma unroll
        for (int i = 0; i < 4; ++i) { float t = __builtin_ldexpf((float)((int)v[j][i] >> 12), ex); t = tr16(t, t); qf[i] = tr32(t, t); }
        if (lane < 16) *(u32x2*)&L.qs[j * 64 + lane * 4] = u32x2{pk(qf[0], qf[1]), pk(qf[2], qf[3])};
      }
      if (lane == 0) *(u32x4*)L.tag = T;
      lds_post(&L.cnt[1], lane);
    } else if (a == 3) {
      st128(T, rws, (s == (h & 7) && lane < 32) ? WS_PR + (unsigned)h * 256u + (lane & 15) * 16u + (lane >> 4) * TWIN : OOB);
      asm volatile("" ::"v"(pf[0]), "v"(pf[1]), "v"(pf[2]));
    }
    asm volatile(".Lcode_end2:");
    UNDEF8(wt);
  }

  const int wave0 = wave, tk1 = tk;
  {
    int lane = threadIdx.x & 63, wave = wave0, b = blockIdx.x, tk = tk1;
    asm volatile("" : "+v"(lane));
    asm volatile("" : "+s"(wave), "+s"(b), "+s"(tk));
    const int g = lane >> 3, c = lane & 7, h = b >> 3, s = b & 7, a = wave - NSW;
    const bool stream = wave < NSW;
    KARGS;
    const gptr w1 = ARG(5), w3 = ARG(6), w2 = ARG(7), out = ARG(8), ws = ARG(9);
    const rsrc_t rws = mk(ws, WS_END), rx = mk(ARG(0), D * 2);
    auto near = [&]() { lds_wait(&L.near, 1); return (unsigned)__builtin_amdgcn_readfirstlane(L.near) - 1u; };
    float fw = 0.f;
    if (a == 3) {
      const unsigned half = (__builtin_amdgcn_s_getreg(20 | (3 << 11)) >> 2) & 1u;
      while (__builtin_amdgcn_s_memrealtime() < tk0 + PT) __builtin_amdgcn_s_sleep(8);
      unsigned tt[2] = {~0u, ~0u};
#pragma unroll
      for (int r = 0; r < 4; ++r) {
        unsigned long long t0, t1;
        unsigned x;
        asm volatile("s_memtime %0\n\ts_waitcnt lgkmcnt(0)\n\ts_load_dword %2, %3, %4\n\ts_waitcnt lgkmcnt(0)\n\ts_memtime %1\n\ts_waitcnt lgkmcnt(0)"
                     : "=&s"(t0), "=&s"(t1), "=&s"(x) : "s"(ws + (WS_PR + h * 256) / 2), "n"((r >> 1) * 128 + (r & 1) * TWIN) : "memory");
        tt[r & 1] = min(tt[r & 1], (unsigned)(t1 - t0));
      }
      const int dt = max(-256, min(256, (int)(tt[0] - tt[1])));
      lds_wait(&L.cnt[0], NSW);
      const f32x2 mlw = *(const f32x2*)L.ml[c];
      const float mwg = asf(asu(max8(mlw[0])) & ~0x3FFu);
      const float lwg = sum8(mlw[1] * __builtin_amdgcn_exp2f((mlw[0] - mwg) * L2E));
      const u32x4 own = tagged(f32x4{mwg, lwg, (float)(half ? -dt : dt), 0.f}, T);
      st128(own, rws, (lane == 0 ? WS_ML + (unsigned)b * 128u : OOB));
      f32x4 mo[1];
      gather<0, MS>(rws, ((lane < 8 && lane != s) ? WS_ML + (unsigned)(h * 8 + lane) * 128u : OOB), T, mo);
      if (lane == s) mo[0] = asf4(own ^ T);
      const unsigned pol = sum8(mo[0][2]) > 0.f;
      if (lane == 0) __hip_atomic_store(&L.near, 1u + ((pol ^ half) & 1u) * TWIN, __ATOMIC_RELAXED, __HIP_MEMORY_SCOPE_WORKGROUP);
      const float mh = max8(mo[0][0]);
      fw = __builtin_amdgcn_exp2f((mlw[0] - mh) * L2E) * __builtin_amdgcn_rcpf(sum8(mo[0][1] * __builtin_amdgcn_exp2f((mo[0][0] - mh) * L2E)));
    }
    if (a == 3) {
      const int dq = min(lane, 47);
      unsigned src = LOFF(L.ored[0][4 * dq]);
      unsigned so = (lane < 48 ? WS_O + (unsigned)s * O_ROW + orow((unsigned)h * (DH * 4) + lane * 16u) : OOB);
      float f[NSW];
#pragma unroll
      for (int w = 0; w < NSW; ++w) { f[w] = rdl(fw, w); asm volatile("" : "+s"(f[w])); }
      asm volatile("" : "+v"(src), "+v"(so));
      f32x4 t[NSW];
      if (tk >= NF + NP) {
        bar();
#pragma unroll
        for (int w = 0; w < NSW; ++w) t[w] = *LAT(const f32x4, src + w * DH * 4);
      } else {
        await<2>(&L.flag[c], 64);
#pragma unroll
        for (int w = 0; w < NSW; ++w) t[w] = *LAT(const f32x4, src + w * DH * 4);
      }
#pragma unroll
      for (int w = 0; w < NSW; ++w) t[w] *= f[w];
      const f32x4 o = ((t[0] + t[1]) + (t[2] + t[3])) + ((t[4] + t[5]) + (t[6] + t[7]));
      const u32x4 ot = tagged(o, T);
      st128(ot, rws, so);
      st128(ot, rws, so + TWIN);
    }
    if (tk < NF) {
      if (wave < 10) {
        u32x4 wb[16];
        unsigned xv;
        {
          const rsrc_t rw2 = mk(w2, FF * D * 2);
          xv = __builtin_amdgcn_raw_buffer_load_b16(rx, (unsigned)(64 * wave + hrow(lane)) * 2u, 0, 0);
          if (!stream) {
            const gptr pw = ((lane & 8) ? w3 : w1) + (64 * wave + hrow(klane(lane))) * FF + 64 * tk + 8 * c;
#pragma unroll
            for (int t = 0; t < 16; ++t) {
              VMWAIT(DW - 1);
              wb[t] = __builtin_nontemporal_load((const __attribute__((address_space(1))) u32x4*)(pw + hrow(krow(t)) * FF));
              SCHED();
            }
            VMWAIT(16);
            lds_post(&L.cnt[3], lane, 256u);
            await<8>(&L.flag[c], 8);
#pragma unroll
            for (int t = 0; t < 8; ++t) {
              VMWAIT(WW - 1);
              wt[t] = ld128<AUX_NT>(rw2, (unsigned)(klane(lane) + (lane & 8)) * 1280u + c * 16u, (64 * tk + krow2(t)) * 1280 + wave * 128);
              SCHED();
            }
          }
          if (stream) {
            lds_wait(&L.cnt[3], 512);
#pragma unroll
            for (int t = 0; t < 16; ++t) wb[t] = L.wst[(lane >> 3) & 1][64 * wave + hrow(klane(lane)) + hrow(krow(t))][c];
          }
        }
        u32x8 b1[8], b2[4];
        turn(wb, b1); turn2(wt, b2);
        SCHED();
        T = *(const u32x4*)L.tag;
        int idx = smf_idx(lane & 8), idx2 = smf_idx(lane & 1);
        unsigned fdst = LOFF(L.fred[wave][(lane >> 5) * 64 + 2 * c + (g & 1)]);
        {
          const u32x4 ones = {0x3F803F80u, 0x3F803F80u, 0x3F803F80u, 0x3F803F80u};
#pragma unroll
          for (int i = 0; i < 4; ++i) *LAT(float, fdst + (LOFF(L.cred) - LOFF(L.fred)) + i * 64) = tr32(smf(ones, b1[i], idx), smf(ones, b1[i + 4], idx));
        }
        raise(&L.flag[8 + wave]);
        f32x4 pv[3];
        float xf = lo16(xv);
        if (lane & 16) fdst = LOFF(L.pred);
        unsigned hdst = (lane == 0 || lane == 40) ? LOFF(L.hsum[0][2 * wave + (g & 1)]) : LOFF(L.pred), asrc = LOFF(L.act[(lane & 48) + 4 * (lane & 2)]);
        unsigned as0 = lane & 8 ? LOFF(L.act[64]) : asrc, as1 = lane & 8 ? asrc : LOFF(L.act[64]);
        unsigned so = ((lane & 8) == 0 ? WS_P3 + prow(tk) + (lane & 16) * (TWIN / 16) + (unsigned)wave * 256u + (lane >> 5) * 128u + c * 16u : OOB);
        asm volatile("" : "+v"(fdst), "+v"(hdst), "+v"(xf), "+v"(as0), "+v"(as1), "+v"(so), "+v"(idx), "+v"(idx2));
        f32x4 d[8], e[2];
        zero(d);
        u32x4 ones = {0x3F803F80u, 0x3F803F80u, 0x3F803F80u, 0x3F803F80u};
        asm volatile("" : "+v"(ones));
        gather<32768, PS + 256 * PLS + 65536 * PK>(rws, WS_P2 + near() + prow(lane >> 4) + (unsigned)wave * 256u + (unsigned)(lane & 15) * 16u, T, pv);
        {
          const f32x4 ps = (pv[0] + pv[1]) + pv[2];
          const float at = tr16(tr32(ps[0], ps[2]), tr32(ps[1], ps[3]));
          const float hv = bfr(xf + bfr(at));
          const unsigned hp = phi(asu(hv), asu(dpp<XOR1>(hv)));
          const u32x4 a0 = {hp, shl<2>(hp), shl<4>(hp), shl<6>(hp)};
          const float s1 = mfma(a0, ones), s2 = mfma(a0, a0);
          SCHED();
#pragma unroll
          for (int i = 0; i < 2; ++i) d[i] = smf4(a0, b1[(i >> 1) + 4 * (i & 1)], d[i], idx);
          SCHED();
          *LAT(float, hdst) = s1; *LAT(float, hdst + 128) = s2;
          SCHED();
#pragma unroll
          for (int i = 2; i < 8; ++i) d[i] = smf4(a0, b1[(i >> 1) + 4 * (i & 1)], d[i], idx);
#pragma unroll
          for (int i = 0; i < 4; ++i) *LAT(float, fdst + i * 64) = tr32(d[2 * i][0], d[2 * i + 1][0]);
        }
        bar();
        zero(e);
        bar();
        const u32x4 a0 = *LAT(const u32x4, as0), a1 = *LAT(const u32x4, as1);
        f32x4 d0 = smf4(a0, b2[0], e[0], idx2), d1 = smf4(a0, b2[1], e[1], idx2);
        SCHED();
        d0 = smf4(a1, b2[2], d0, idx2); d1 = smf4(a1, b2[3], d1, idx2);
        st128(tagged(f32x4{d0[0] + dpp<ROR8>(d0[2]), d0[1] + dpp<ROR8>(d0[3]), d1[0] + dpp<ROR8>(d1[2]), d1[1] + dpp<ROR8>(d1[3])}, T), rws, so);
        LEAVE();
      }
    } else if (a == 3 && tk >= NF + NP && tk < NF + NP + NR) {
      const int j = tk - NF - NP, col0 = 64 * (j >> 3) + 16 * (j & 3) + (j & 4) + 8 * (lane & 1), e = 2 * (lane >> 5) + ((lane >> 4) & 1);
      const unsigned xv = __builtin_amdgcn_raw_buffer_load_b16(rx, (unsigned)(col0 + e) * 2u, 0, 0);
      const unsigned fo = near() + prow(lane >> 1);
      auto fold = [&](f32x4 f) {
        const float r = tr16(tr32(f[0], f[2]), tr32(f[1], f[3]));
        return sum21(r + dpp<ROR8>(r));
      };
      f32x4 pv[1];
      {
        while (__builtin_amdgcn_s_memrealtime() < tk0 + G3) __builtin_amdgcn_s_sleep(2);
        const unsigned x = ld32<AUX_SC1>(rws, lane < 48 ? WS_P3 + fo + (unsigned)j * 32u : OOB, 0);
        asm volatile("" ::"v"(x));
      }
      while (__builtin_amdgcn_s_memrealtime() < tk0 + GR) __builtin_amdgcn_s_sleep(2);
      gather<0, HS>(rws, (lane < 24 ? WS_P2 + fo + (unsigned)col0 * 4u : OOB), T, pv);
      float hsel = bfr(lo16(xv) + bfr(fold(pv[0])));
      unsigned so = ((lane & 14) == 0 ? (unsigned)(col0 + e) * 2u : OOB), se = ((tk == NF + NP && lane == 0) ? WS_EPOCH : OOB);
      asm volatile("" : "+v"(hsel), "+v"(so), "+v"(se));
      gather<0, RS>(rws, (lane < 48 ? WS_P3 + fo + (unsigned)j * 32u + (lane & 1) * 16u : OOB), T, pv);
      __builtin_amdgcn_raw_buffer_store_b16((unsigned short)(pk(hsel + bfr(fold(pv[0])), 0.f) & 0xFFFFu), mk(out, D * 2), so, 0, AUX_SC1 | 1);
      st64(e1, rws, se);
      LEAVE();
    }
    if (a == 2) {
      if (tk < NF) {
        const int li = c * 16 + g * 2;
        if (lane < 4) *(unsigned*)&L.act[64 + 2 * lane] = 0u;
        await<2>(&L.flag[8 + (lane & 15)], 64);
        f32x2 cs = {0.f, 0.f};
#pragma unroll
        for (int j = 0; j < 10; ++j) cs += *(const f32x2*)&L.cred[j][li];
        unsigned src = LOFF(L.fred[0][li]), adst = LOFF(L.act[lane]);
        asm volatile("" : "+v"(cs), "+v"(src), "+v"(adst));
        unsigned* const hs = &L.hsum[0][lane];
        POLL_LOOP {
          if (__builtin_popcountll(__builtin_amdgcn_ballot_w64(__hip_atomic_load(hs, __ATOMIC_RELAXED, __HIP_MEMORY_SCOPE_WORKGROUP) != ~0u)) >= 28) break;
          __builtin_amdgcn_s_sleep(2);
        }
        unsigned sv, r0, r1, r2, r3, cap = SPIN_CAP;
#define LOOK(N, O) "ds_read_b32 %" #N ", %6\n\ts_waitcnt lgkmcnt(3)\n\tv_cmp_eq_u32 vcc, -1, %" #O "\n\tv_mov_b32 %0, %" #O "\n\ts_cbranch_vccz 2f\n\t"
        asm volatile("ds_read_b32 %1, %6\n\tds_read_b32 %2, %6\n\tds_read_b32 %3, %6\n"
                     "1:\n\t" LOOK(4, 1) LOOK(1, 2) LOOK(2, 3) LOOK(3, 4) "s_sub_u32 %5, %5, 1\n\ts_cbranch_scc0 1b\n"
                     "2:"
                     : "=&v"(sv), "=&v"(r0), "=&v"(r1), "=&v"(r2), "=&v"(r3), "+s"(cap) : "v"(LOFF(*hs) + (unsigned)(size_t)&L) : "vcc", "scc", "memory");
        SCHED();
        f32x2 t[10];
        asm volatile("s_barrier" ::: "memory");
        SCHED();
#pragma unroll
        for (int j = 0; j < 10; ++j) t[j] = *LAT(const f32x2, src + j * 512);
        SCHED();
        float s1 = sum8(asf(sv));
        s1 += dpp<ROR8>(s1);
        s1 = tr16(s1, s1);
        const auto s12 = __builtin_amdgcn_permlane32_swap(asu(s1), asu(s1), false, false);
        const float mean = asf(s12[0]) * (1.f / D), s2 = asf(s12[1]);
        float rden = rstd(bfr(fmaxf(s2 * (1.f / D) - mean * mean, 0.f)));
        cs *= -bfr(mean);
        asm volatile("" : "+v"(cs), "+v"(rden));
        SCHED();
        const f32x2 gu = ((((t[0] + t[1]) + (t[2] + t[3])) + ((t[4] + t[5]) + (t[6] + t[7]))) + ((t[8] + t[9]) + cs)) * rden;
        asm volatile("" ::"v"(r0), "v"(r1), "v"(r2), "v"(r3), "v"(gu));
        const unsigned gp = pk(gu[0], gu[1]);
        const float gt = lo16(gp);
        *LAT(unsigned short, adst) = (unsigned short)(pk(bfr(gt * __builtin_amdgcn_rcpf(1.f + __builtin_amdgcn_exp2f(-gt * L2E))) * hi16(gp), 0.f) & 0xFFFFu);
        bar();
        LEAVE();
      }
      if (tk >= NF && tk < NF + NP) {
        unsigned src = LOFF(L.pred[0][4 * (lane & 15)]);
        unsigned so = (lane < 32 ? WS_P2 + prow((tk - NF) / 10) + (lane & 16) * (TWIN / 16) + (unsigned)((tk - NF) % 10) * 256u + (lane & 15) * 16u : OOB);
        asm volatile("" : "+v"(src), "+v"(so));
        f32x4 t[NSW];
        bar();
#pragma unroll
        for (int w = 0; w < NSW; ++w) t[w] = *LAT(const f32x4, src + w * 256);
        st128(tagged(((t[0] + t[1]) + (t[2] + t[3])) + ((t[4] + t[5]) + (t[6] + t[7])), T), rws, so);
        LEAVE();
      }
    }
    if (!stream) {
      if (tk < NF) { if (a == 3) { bar(); bar(); } }
      else if (tk < NF + NP ? a != 2 : a != 3) bar();
      LEAVE();
    }
  }
  asm volatile(".Lcode_end:");
}

}

extern "C" void kernel_launch(void* const* d_in, const int* in_sizes, int n_in, void* d_out, int out_size, void* d_ws, size_t ws_size,
                              hipStream_t stream) {
  Plan plan;
  for (int b = 0; b < NWG; ++b) {
    const int h = b >> 3, s = b & 7, e = (s - h - 1) & 7;
    const bool big = h >= NF, slow = !big && (h & 7) == 3;
    const int kp = big ? 512 : slow ? KP2 : KP1, kn = big ? 512 : slow ? KN2 : KN1, kf = WIN - 2 * kn - 5 * kp, nk = e == 7 ? kf : e < 2 ? kn : kp;
    int k0 = 0;
    for (int j = 0; j != e; ++j) k0 += j < 2 ? kn : kp;
    const int qi = big ? 96 + s * 8 + h - NF : e < 2 ? 2 * h + e : 46 + (slow ? 21 + (h >> 3) : h - ((h + 4) >> 3)) * 5 + e;
    const bool qjob = (big || e != 7) && qi < 160;
    const int q2 = qi - 96, q3 = qi < 32 ? qi : (qi - 32) >> 1;
    const int qline = q2 >= 0 ? q2 + q2 / 7 + (q2 % 7 >= 3) : q3 < 9 ? q3 * 8 + 3 : 64 + q3, qpart = q2 >= 0 ? !big : qi < 32 ? 2 : qi & 1;
    const int nl = big ? NB : q2 >= 0 ? 20 - NB : qpart == 2 ? 8 : 6, row0 = (q2 >= 0 ? 32 * NB : 192) * qpart;
    plan.w[3 * b] = (unsigned)(h * SK + (SK - WIN) + k0) * (DH * 2);
    plan.w[3 * b + 1] = qjob ? (unsigned)(row0 * 12288 + qline * 128) : OOB;
    plan.w[3 * b + 2] = (unsigned)(nk | (qjob ? nl << 10 | qpart << 14 : 0) | job_of(h, s) << 16);
  }
  plan.w[3 * NWG] = plan.w[3 * NWG + 1] = 0u;
  hipLaunchKernelGGL(k_fused, dim3(NWG), dim3(NTHR), 0, stream, (const unsigned short*)d_in[0], (const unsigned short*)d_in[1],
                     (const unsigned short*)d_in[2], (const unsigned short*)d_in[3], (const unsigned short*)d_in[4],
                     (const unsigned short*)d_in[6], (const unsigned short*)d_in[7], (const unsigned short*)d_in[8],
                     (unsigned short*)d_out, (unsigned char*)d_ws, plan);
}
```

```cpp
#include <hip/hip_runtime.h>
#include <cstdint>

namespace {

constexpr int D = 640, DH = 192, SK = 32768, WIN = 4096, FF = 1536;
constexpr int NWG = 256, NSW = 8, NTHR = 768;
constexpr int KP1 = 544;
constexpr int KN1 = 576;
constexpr int KP2 = 560;
constexpr int KN2 = 576;
constexpr int NB = 12;
constexpr int KW = 6;
constexpr int VW = 6;
constexpr int VN = 4;
constexpr int VP = 6;
constexpr int WS = 3;
constexpr int WS2 = 2;
constexpr int D2 = 30;
constexpr int HP = 48;
constexpr int WW = 4;
constexpr int W2 = 5;
constexpr int DW = 8;
constexpr int SA = 80;
constexpr int SP = 1490;
constexpr int SE = 1570;
constexpr int SW = 1650;
constexpr int TW = 20;
constexpr int PT = 580;
constexpr int QT = 630;
constexpr int G3 = 1810;
constexpr int GR = 1880;
constexpr int QS = 10;
constexpr int MS = 4;
constexpr int OS = 1;
constexpr int OL = 8;
constexpr int OK = 32;
constexpr int PS = 0;
constexpr int PLS = 6;
constexpr int PK = 64;
constexpr int HS = 1;
constexpr int RS = 0;
constexpr int CPF = 16;
static_assert(KW == 6 && WIN - 2 * KN1 - 5 * KP1 >= 128 && WIN - 2 * KN2 - 5 * KP2 >= 128, "the first two key groups are never cut");

constexpr int NF = 24, NP = 120, NR = 80;
constexpr unsigned WS_EPOCH = 0;
constexpr unsigned TWIN = 8192;
__device__ __forceinline__ unsigned prow(unsigned r) { return (r >> 1) * 16384u + (r & 1) * 4096u; }
__device__ __forceinline__ unsigned orow(unsigned o) { return (o >> 13) * 16384u + (o & 8191u); }
constexpr unsigned WS_PR = 16384;
constexpr unsigned WS_P2 = 32768;
constexpr unsigned WS_P3 = WS_P2 + 6 * 16384;
constexpr unsigned O_ROW = 3 * 16384;
constexpr unsigned WS_O = WS_P3 + 12 * 16384;
constexpr unsigned WS_ML = WS_O + 8 * O_ROW;
constexpr unsigned WS_Q = WS_ML + 256 * 128;
constexpr unsigned WS_END = WS_Q + 96 * 1024;
constexpr unsigned OOB = 0x7F000000u;
constexpr int SPIN_CAP = 1 << 17;

typedef unsigned u32x2 __attribute__((ext_vector_type(2)));
typedef unsigned u32x4 __attribute__((ext_vector_type(4)));
typedef unsigned u32x8 __attribute__((ext_vector_type(8)));
typedef float f32x2 __attribute__((ext_vector_type(2)));
typedef float f32x4 __attribute__((ext_vector_type(4)));
typedef __bf16 bf16x2 __attribute__((ext_vector_type(2)));
typedef __bf16 bf16x8 __attribute__((ext_vector_type(8)));
typedef __bf16 bf16x16 __attribute__((ext_vector_type(16)));
typedef __amdgpu_buffer_rsrc_t rsrc_t;
constexpr int AUX_NT = 2, AUX_SC1 = 16;

template <class P> __device__ __forceinline__ rsrc_t mk(P p, unsigned bytes) { return __builtin_amdgcn_make_buffer_rsrc((void*)p, 0, bytes, 0x00020000); }
template <int AUX> __device__ __forceinline__ u32x4 ld128(rsrc_t r, unsigned vo, unsigned so) { return __builtin_amdgcn_raw_buffer_load_b128(r, vo, so, AUX); }
template <int AUX> __device__ __forceinline__ u32x2 ld64(rsrc_t r, unsigned vo, unsigned so) { return __builtin_amdgcn_raw_buffer_load_b64(r, vo, so, AUX); }
template <int AUX> __device__ __forceinline__ unsigned ld32(rsrc_t r, unsigned vo, unsigned so) { return __builtin_amdgcn_raw_buffer_load_b32(r, vo, so, AUX); }
__device__ __forceinline__ void st128(u32x4 v, rsrc_t r, unsigned vo) { __builtin_amdgcn_raw_buffer_store_b128(v, r, vo, 0, AUX_SC1); }
__device__ __forceinline__ void st64(u32x2 v, rsrc_t r, unsigned vo) { __builtin_amdgcn_raw_buffer_store_b64(v, r, vo, 0, AUX_SC1); }

__device__ __forceinline__ float asf(unsigned u) { return __uint_as_float(u); }
__device__ __forceinline__ unsigned asu(float f) { return __float_as_uint(f); }
__device__ __forceinline__ f32x4 asf4(u32x4 v) { return __builtin_bit_cast(f32x4, v); }
__device__ __forceinline__ float lo16(unsigned u) { return asf(u << 16); }
__device__ __forceinline__ float hi16(unsigned u) { return asf(u & 0xFFFF0000u); }
__device__ __forceinline__ unsigned pk(float lo, float hi) { bf16x2 v = {(__bf16)lo, (__bf16)hi}; return __builtin_bit_cast(unsigned, v); }
__device__ __forceinline__ float bfr(float f) { return (float)(__bf16)f; }
__device__ __forceinline__ float dot2(unsigned a, unsigned b, float acc) {
  return __builtin_amdgcn_fdot2_f32_bf16(__builtin_bit_cast(bf16x2, a), __builtin_bit_cast(bf16x2, b), acc, false);
}
__device__ __forceinline__ f32x4 smf4(u32x4 a, u32x8 b, f32x4 c, int idx) {
  return __builtin_amdgcn_smfmac_f32_16x16x64_bf16(__builtin_bit_cast(bf16x8, a), __builtin_bit_cast(bf16x16, b), c, idx, 0, 0);
}
__device__ __forceinline__ float smf(u32x4 a, u32x8 b, int idx) { return smf4(a, b, f32x4{0.f, 0.f, 0.f, 0.f}, idx)[0]; }
__device__ __forceinline__ float mfma(u32x4 a, u32x4 b) {
  return __builtin_amdgcn_mfma_f32_16x16x32_bf16(__builtin_bit_cast(bf16x8, a), __builtin_bit_cast(bf16x8, b), f32x4{0.f, 0.f, 0.f, 0.f}, 0, 0, 0)[0];
}
template <int N> __device__ __forceinline__ void zero(f32x4 (&d)[N]) {
#pragma unroll
  for (int i = 0; i < N; ++i) { d[i] = f32x4{0.f, 0.f, 0.f, 0.f}; asm volatile("" : "+v"(d[i])); }
}
__device__ __forceinline__ int smf_idx(int odd) { return odd ? 0xEEEE : 0x4444; }
constexpr int hrow(int i) { return 4 * (i & 15) + 2 * (i >> 5) + ((i >> 4) & 1); }
constexpr int krow(int j) { return 32 * (j >> 3) + 8 * ((j >> 1) & 1) + 2 * ((j >> 2) & 1) + (j & 1); }
__device__ __forceinline__ int klane(int lane) { return 16 * (lane >> 5) + 4 * ((lane >> 4) & 1); }
__device__ __forceinline__ unsigned plo(unsigned a, unsigned b) { return __builtin_amdgcn_perm(b, a, 0x05040100u); }
__device__ __forceinline__ unsigned phi(unsigned a, unsigned b) { return __builtin_amdgcn_perm(b, a, 0x07060302u); }
template <int CTRL> __device__ __forceinline__ float dpp(float x) {
  return __builtin_bit_cast(float, __builtin_amdgcn_mov_dpp(__builtin_bit_cast(int, x), CTRL, 0xf, 0xf, true));
}
template <int N> __device__ __forceinline__ unsigned shl(unsigned x) { return (unsigned)__builtin_amdgcn_mov_dpp((int)x, 0x100 + N, 0xf, 0xf, true); }
constexpr int XOR1 = 0xB1, XOR2 = 0x4E, HMIR = 0x141, ROR8 = 0x128, SHL4 = 0x104;
__device__ __forceinline__ float tr32(float a, float b) { auto s = __builtin_amdgcn_permlane32_swap(asu(a), asu(b), false, false); return asf(s[0]) + asf(s[1]); }
__device__ __forceinline__ float tr16(float a, float b) { auto s = __builtin_amdgcn_permlane16_swap(asu(a), asu(b), false, false); return asf(s[0]) + asf(s[1]); }
__device__ __forceinline__ float tr8(float a, float b, bool b3) { const float t1 = a + dpp<ROR8>(a), t2 = b + dpp<ROR8>(b); return b3 ? t2 : t1; }
__device__ __forceinline__ float sum8(float x) { x += dpp<XOR1>(x); x += dpp<XOR2>(x); x += dpp<HMIR>(x); return x; }
__device__ __forceinline__ float max8(float x) { x = fmaxf(x, dpp<XOR1>(x)); x = fmaxf(x, dpp<XOR2>(x)); x = fmaxf(x, dpp<HMIR>(x)); return x; }
__device__ __forceinline__ float sumg(float x) { x += dpp<ROR8>(x); x = tr16(x, x); return tr32(x, x); }
__device__ __forceinline__ float maxg(float x) {
  x = fmaxf(x, dpp<ROR8>(x));
  auto s = __builtin_amdgcn_permlane16_swap(asu(x), asu(x), false, false); x = fmaxf(asf(s[0]), asf(s[1]));
  auto t = __builtin_amdgcn_permlane32_swap(asu(x), asu(x), false, false); return fmaxf(asf(t[0]), asf(t[1]));
}
__device__ __forceinline__ float sum64(float x) { return sumg(sum8(x)); }
__device__ __forceinline__ float sum21(float x) { x += dpp<SHL4>(x); x += dpp<XOR2>(x); return x; }
__device__ __forceinline__ float rdl(float v, int l) { return asf((unsigned)__builtin_amdgcn_readlane((int)asu(v), l)); }
__device__ __forceinline__ float rstd(float var) { return __builtin_amdgcn_rcpf(bfr(__builtin_amdgcn_sqrtf(bfr(var + 1e-5f)))); }
__device__ __forceinline__ void bar() {
  __builtin_amdgcn_sched_barrier(0);
  asm volatile("s_waitcnt lgkmcnt(0)\n\ts_barrier" ::: "memory");
  __builtin_amdgcn_sched_barrier(0);
}
#define SCHED() __builtin_amdgcn_sched_barrier(0)
#define LEAVE() asm volatile("s_endpgm" ::: "memory")
#define POLL_LOOP _Pragma("nounroll") for (int spin = 0; spin < SPIN_CAP; ++spin)
#define VMWAIT(n) asm volatile("s_waitcnt vmcnt(%0)" ::"n"(n) : "memory")
#define PACE(W, STEP) asm volatile("s_waitcnt vmcnt(%2) lgkmcnt(0)\n\ts_lshl_b32 s94, s92, 4\n\ts_sub_u32 s94, s94, %0\n\ts_add_u32 %0, %0, %1\n\ts_cmp_lt_i32 s94, 0\n\ts_cbranch_scc0 1f\n\t" \
    "s_waitcnt vmcnt(%3)\n\ts_cmp_lt_i32 s94, %5\n\ts_cbranch_scc0 1f\n\ts_waitcnt vmcnt(%4)\n1:\n\ts_memrealtime s[92:93]" \
    : "+s"(due) : "s"(STEP), "n"((W) - 1), "n"(WS - 1), "n"(WS2 - 1), "n"(-16 * D2) : "s92", "s93", "s94", "scc", "memory")
#define HARD(W, STEP) asm volatile("s_waitcnt vmcnt(%2)\n\ts_add_u32 %0, %0, %1\n1:\n\ts_waitcnt lgkmcnt(0)\n\ts_lshl_b32 s94, s92, 4\n\ts_memrealtime s[92:93]\n\ts_sub_u32 s94, s94, %0\n\t" \
    "s_cmp_lt_i32 s94, 0\n\ts_cbranch_scc1 1b" : "+s"(due) : "s"(STEP), "n"((W) - 1) : "s92", "s93", "s94", "scc", "memory")
#define GATE(W, STEP, I) do { if ((I) >= HP) HARD(W, STEP); else PACE(W, STEP); } while (0)
constexpr float L2E = 1.4426950408889634f;

__device__ __forceinline__ u32x4 make_tag(u32x2 e1) {
  const unsigned a0 = e1[0] & 0x3FFu;
  return u32x4{a0, ((e1[0] >> 10) + a0) & 0x3FFu, ((e1[0] >> 20) + a0) & 0x3FFu, (((e1[0] >> 30) | (e1[1] << 2)) + a0) & 0x3FFu};
}
__device__ __forceinline__ u32x4 tagged(f32x4 o, u32x4 T) {
  u32x4 r;
#pragma unroll
  for (int k = 0; k < 4; ++k) r[k] = __builtin_amdgcn_bitop3_b32(asu(o[k]), ~0x3FFu, T[k], 0xEA);
  return r;
}
template <int N, int S, int MODE, int K> __device__ __forceinline__ void poll(rsrc_t r, const unsigned (&off)[N], u32x4 T, u32x4 (&v)[N]);
template <> __device__ __forceinline__ void poll<1, 0, 0, 0>(rsrc_t r, const unsigned (&off)[1], u32x4 T, u32x4 (&v)[1]) {
  asm volatile(
      "s_mov_b64 s[88:89], exec\ns_mov_b32 s90, 0x20000\nv_cmp_ne_u32 vcc, 0x7f000000, %1\ns_mov_b64 s[80:81], vcc\n.Lp_%=:\ns_mov_b64 exec, s[80:81]\n"
      "s_cbranch_execz .Ls0_%=\nbuffer_load_dwordx4 v[160:163], %1, %6, 0 offen sc1\n.Ls0_%=:\ns_mov_b64 exec, s[88:89]\ns_waitcnt vmcnt(0)\nv_xor_b32 v164, v161, %3\n"
      "v_bitop3_b32 v164, v162, %4, v164 bitop3:0xbe\nv_bitop3_b32 v164, v163, %5, v164 bitop3:0xbe\nv_bitop3_b32 v164, v160, %2, v164 bitop3:0xbe\n"
      "v_and_b32 v164, 0x3ff, v164\nv_cmp_ne_u32 vcc, 0, v164\ns_and_b64 s[80:81], s[80:81], vcc\ns_cbranch_scc0 .Le_%=\ns_sub_u32 s90, s90, 1\ns_cbranch_scc0 .Lp_%=\n"
      ".Le_%=:\n"
      : "+&{v[160:163]}"(v[0])
      : "v"(off[0]), "v"(T[0]), "v"(T[1]), "v"(T[2]), "v"(T[3]), "s"(r)
      : "memory", "vcc", "scc", "v164", "s80", "s81", "s88", "s89", "s90");
}
template <> __device__ __forceinline__ void poll<1, 1, 0, 0>(rsrc_t r, const unsigned (&off)[1], u32x4 T, u32x4 (&v)[1]) {
  asm volatile(
      "s_mov_b64 s[88:89], exec\ns_mov_b32 s90, 0x20000\nv_cmp_ne_u32 vcc, 0x7f000000, %1\ns_mov_b64 s[80:81], vcc\n.Lp_%=:\ns_mov_b64 exec, s[80:81]\n"
      "s_cbranch_execz .Ls0_%=\nbuffer_load_dwordx4 v[160:163], %1, %6, 0 offen sc1\n.Ls0_%=:\ns_mov_b64 exec, s[88:89]\ns_waitcnt vmcnt(0)\nv_xor_b32 v164, v161, %3\n"
      "v_bitop3_b32 v164, v162, %4, v164 bitop3:0xbe\nv_bitop3_b32 v164, v163, %5, v164 bitop3:0xbe\nv_bitop3_b32 v164, v160, %2, v164 bitop3:0xbe\n"
      "v_and_b32 v164, 0x3ff, v164\nv_cmp_ne_u32 vcc, 0, v164\ns_and_b64 s[80:81], s[80:81], vcc\ns_cbranch_scc0 .Le_%=\ns_sleep 1\ns_sub_u32 s90, s90, 1\n"
      "s_cbranch_scc0 .Lp_%=\n.Le_%=:\n"
      : "+&{v[160:163]}"(v[0])
      : "v"(off[0]), "v"(T[0]), "v"(T[1]), "v"(T[2]), "v"(T[3]), "s"(r)
      : "memory", "vcc", "scc", "v164", "s80", "s81", "s88", "s89", "s90");
}
template <> __device__ __forceinline__ void poll<1, 4, 0, 0>(rsrc_t r, const unsigned (&off)[1], u32x4 T, u32x4 (&v)[1]) {
  asm volatile(
      "s_mov_b64 s[88:89], exec\ns_mov_b32 s90, 0x20000\nv_cmp_ne_u32 vcc, 0x7f000000, %1\ns_mov_b64 s[80:81], vcc\n.Lp_%=:\ns_mov_b64 exec, s[80:81]\n"
      "s_cbranch_execz .Ls0_%=\nbuffer_load_dwordx4 v[160:163], %1, %6, 0 offen sc1\n.Ls0_%=:\ns_mov_b64 exec, s[88:89]\ns_waitcnt vmcnt(0)\nv_xor_b32 v164, v161, %3\n"
      "v_bitop3_b32 v164, v162, %4, v164 bitop3:0xbe\nv_bitop3_b32 v164, v163, %5, v164 bitop3:0xbe\nv_bitop3_b32 v164, v160, %2, v164 bitop3:0xbe\n"
      "v_and_b32 v164, 0x3ff, v164\nv_cmp_ne_u32 vcc, 0, v164\ns_and_b64 s[80:81], s[80:81], vcc\ns_cbranch_scc0 .Le_%=\ns_sleep 4\ns_sub_u32 s90, s90, 1\n"
      "s_cbranch_scc0 .Lp_%=\n.Le_%=:\n"
      : "+&{v[160:163]}"(v[0])
      : "v"(off[0]), "v"(T[0]), "v"(T[1]), "v"(T[2]), "v"(T[3]), "s"(r)
      : "memory", "vcc", "scc", "v164", "s80", "s81", "s88", "s89", "s90");
}
template <> __device__ __forceinline__ void poll<2, 2099201, 5, 0>(rsrc_t r, const unsigned (&off)[2], u32x4 T, u32x4 (&v)[2]) {
  asm volatile(
      "s_mov_b64 s[88:89], exec\ns_mov_b32 s90, 0x20000\nv_cmp_ne_u32 vcc, 0x7f000000, %2\ns_mov_b64 s[80:81], vcc\nv_cmp_ne_u32 vcc, 0x7f000000, %3\n"
      "s_mov_b64 s[82:83], vcc\ns_branch .Lc_%=\n.Lp_%=:\ns_mov_b64 exec, s[80:81]\ns_cbranch_execz .Ls0_%=\nbuffer_load_dwordx4 v[156:159], %2, %8, 0 offen sc1\n"
      ".Ls0_%=:\ns_mov_b64 exec, s[82:83]\ns_cbranch_execz .Ls1_%=\nbuffer_load_dwordx4 v[160:163], %3, %8, 0 offen sc1\n.Ls1_%=:\ns_mov_b64 exec, s[88:89]\n"
      "s_waitcnt vmcnt(0)\n.Lc_%=:\nv_xor_b32 v164, v157, %5\nv_bitop3_b32 v164, v158, %6, v164 bitop3:0xbe\nv_bitop3_b32 v164, v159, %7, v164 bitop3:0xbe\n"
      "v_bitop3_b32 v164, v156, %4, v164 bitop3:0xbe\nv_and_b32 v164, 0x3ff, v164\nv_cmp_ne_u32 vcc, 0, v164\ns_and_b64 s[80:81], s[80:81], vcc\n"
      "v_xor_b32 v164, v161, %5\nv_bitop3_b32 v164, v162, %6, v164 bitop3:0xbe\nv_bitop3_b32 v164, v163, %7, v164 bitop3:0xbe\n"
      "v_bitop3_b32 v164, v160, %4, v164 bitop3:0xbe\nv_and_b32 v164, 0x3ff, v164\nv_cmp_ne_u32 vcc, 0, v164\ns_and_b64 s[82:83], s[82:83], vcc\n"
      "s_or_b64 s[92:93], s[80:81], s[82:83]\ns_cbranch_scc0 .Le_%=\ns_bcnt1_i32_b64 s96, s[80:81]\ns_mov_b32 s91, s96\ns_bcnt1_i32_b64 s96, s[82:83]\n"
      "s_add_u32 s91, s91, s96\ns_cmp_le_u32 s91, 32\ns_cbranch_scc1 .Lq_%=\ns_sleep 8\n.Lq_%=:\ns_sleep 1\ns_sub_u32 s90, s90, 1\ns_cbranch_scc0 .Lp_%=\n.Le_%=:\n"
      : "+&{v[156:159]}"(v[0]), "+&{v[160:163]}"(v[1])
      : "v"(off[0]), "v"(off[1]), "v"(T[0]), "v"(T[1]), "v"(T[2]), "v"(T[3]), "s"(r)
      : "memory", "vcc", "scc", "v164", "s80", "s81", "s82", "s83", "s88", "s89", "s90", "s92", "s93", "s91", "s96");
}
template <> __device__ __forceinline__ void poll<3, 10, 0, 0>(rsrc_t r, const unsigned (&off)[3], u32x4 T, u32x4 (&v)[3]) {
  asm volatile(
      "s_mov_b64 s[88:89], exec\ns_mov_b32 s90, 0x20000\nv_cmp_ne_u32 vcc, 0x7f000000, %3\ns_mov_b64 s[80:81], vcc\nv_cmp_ne_u32 vcc, 0x7f000000, %4\n"
      "s_mov_b64 s[82:83], vcc\nv_cmp_ne_u32 vcc, 0x7f000000, %5\ns_mov_b64 s[84:85], vcc\n.Lp_%=:\ns_mov_b64 exec, s[80:81]\ns_cbranch_execz .Ls0_%=\n"
      "buffer_load_dwordx4 v[152:155], %3, %10, 0 offen sc1\n.Ls0_%=:\ns_mov_b64 exec, s[82:83]\ns_cbranch_execz .Ls1_%=\n"
      "buffer_load_dwordx4 v[156:159], %4, %10, 0 offen sc1\n.Ls1_%=:\ns_mov_b64 exec, s[84:85]\ns_cbranch_execz .Ls2_%=\n"
      "buffer_load_dwordx4 v[160:163], %5, %10, 0 offen sc1\n.Ls2_%=:\ns_mov_b64 exec, s[88:89]\ns_waitcnt vmcnt(0)\nv_xor_b32 v164, v153, %7\n"
      "v_bitop3_b32 v164, v154, %8, v164 bitop3:0xbe\nv_bitop3_b32 v164, v155, %9, v164 bitop3:0xbe\nv_bitop3_b32 v164, v152, %6, v164 bitop3:0xbe\n"
      "v_and_b32 v164, 0x3ff, v164\nv_cmp_ne_u32 vcc, 0, v164\ns_and_b64 s[80:81], s[80:81], vcc\nv_xor_b32 v164, v157, %7\n"
      "v_bitop3_b32 v164, v158, %8, v164 bitop3:0xbe\nv_bitop3_b32 v164, v159, %9, v164 bitop3:0xbe\nv_bitop3_b32 v164, v156, %6, v164 bitop3:0xbe\n"
      "v_and_b32 v164, 0x3ff, v164\nv_cmp_ne_u32 vcc, 0, v164\ns_and_b64 s[82:83], s[82:83], vcc\ns_or_b64 s[92:93], s[80:81], s[82:83]\nv_xor_b32 v164, v161, %7\n"
      "v_bitop3_b32 v164, v162, %8, v164 bitop3:0xbe\nv_bitop3_b32 v164, v163, %9, v164 bitop3:0xbe\nv_bitop3_b32 v164, v160, %6, v164 bitop3:0xbe\n"
      "v_and_b32 v164, 0x3ff, v164\nv_cmp_ne_u32 vcc, 0, v164\ns_and_b64 s[84:85], s[84:85], vcc\ns_or_b64 s[92:93], s[92:93], s[84:85]\ns_cbranch_scc0 .Le_%=\n"
      "s_sleep 10\ns_sub_u32 s90, s90, 1\ns_cbranch_scc0 .Lp_%=\n.Le_%=:\n"
      : "+&{v[152:155]}"(v[0]), "+&{v[156:159]}"(v[1]), "+&{v[160:163]}"(v[2])
      : "v"(off[0]), "v"(off[1]), "v"(off[2]), "v"(T[0]), "v"(T[1]), "v"(T[2]), "v"(T[3]), "s"(r)
      : "memory", "vcc", "scc", "v164", "s80", "s81", "s82", "s83", "s84", "s85", "s88", "s89", "s90", "s92", "s93");
}
template <> __device__ __forceinline__ void poll<3, 4195840, 0, 0>(rsrc_t r, const unsigned (&off)[3], u32x4 T, u32x4 (&v)[3]) {
  asm volatile(
      "s_mov_b64 s[88:89], exec\ns_mov_b32 s90, 0x20000\nv_cmp_ne_u32 vcc, 0x7f000000, %3\ns_mov_b64 s[80:81], vcc\nv_cmp_ne_u32 vcc, 0x7f000000, %4\n"
      "s_mov_b64 s[82:83], vcc\nv_cmp_ne_u32 vcc, 0x7f000000, %5\ns_mov_b64 s[84:85], vcc\n.Lp_%=:\ns_mov_b64 exec, s[80:81]\ns_cbranch_execz .Ls0_%=\n"
      "buffer_load_dwordx4 v[152:155], %3, %10, 0 offen sc1\n.Ls0_%=:\ns_mov_b64 exec, s[82:83]\ns_cbranch_execz .Ls1_%=\n"
      "buffer_load_dwordx4 v[156:159], %4, %10, 0 offen sc1\n.Ls1_%=:\ns_mov_b64 exec, s[84:85]\ns_cbranch_execz .Ls2_%=\n"
      "buffer_load_dwordx4 v[160:163], %5, %10, 0 offen sc1\n.Ls2_%=:\ns_mov_b64 exec, s[88:89]\ns_waitcnt vmcnt(0)\nv_xor_b32 v164, v153, %7\n"
      "v_bitop3_b32 v164, v154, %8, v164 bitop3:0xbe\nv_bitop3_b32 v164, v155, %9, v164 bitop3:0xbe\nv_bitop3_b32 v164, v152, %6, v164 bitop3:0xbe\n"
      "v_and_b32 v164, 0x3ff, v164\nv_cmp_ne_u32 vcc, 0, v164\ns_and_b64 s[80:81], s[80:81], vcc\nv_xor_b32 v164, v157, %7\n"
      "v_bitop3_b32 v164, v158, %8, v164 bitop3:0xbe\nv_bitop3_b32 v164, v159, %9, v164 bitop3:0xbe\nv_bitop3_b32 v164, v156, %6, v164 bitop3:0xbe\n"
      "v_and_b32 v164, 0x3ff, v164\nv_cmp_ne_u32 vcc, 0, v164\ns_and_b64 s[82:83], s[82:83], vcc\ns_or_b64 s[92:93], s[80:81], s[82:83]\nv_xor_b32 v164, v161, %7\n"
      "v_bitop3_b32 v164, v162, %8, v164 bitop3:0xbe\nv_bitop3_b32 v164, v163, %9, v164 bitop3:0xbe\nv_bitop3_b32 v164, v160, %6, v164 bitop3:0xbe\n"
      "v_and_b32 v164, 0x3ff, v164\nv_cmp_ne_u32 vcc, 0, v164\ns_and_b64 s[84:85], s[84:85], vcc\ns_or_b64 s[92:93], s[92:93], s[84:85]\ns_cbranch_scc0 .Le_%=\n"
      "s_bcnt1_i32_b64 s96, s[80:81]\ns_mov_b32 s91, s96\ns_bcnt1_i32_b64 s96, s[82:83]\ns_add_u32 s91, s91, s96\ns_bcnt1_i32_b64 s96, s[84:85]\ns_add_u32 s91, s91, s96\n"
      "s_cmp_le_u32 s91, 64\ns_cbranch_scc1 .Lq_%=\ns_sleep 6\n.Lq_%=:\ns_sub_u32 s90, s90, 1\ns_cbranch_scc0 .Lp_%=\n.Le_%=:\n"
      : "+&{v[152:155]}"(v[0]), "+&{v[156:159]}"(v[1]), "+&{v[160:163]}"(v[2])
      : "v"(off[0]), "v"(off[1]), "v"(off[2]), "v"(T[0]), "v"(T[1]), "v"(T[2]), "v"(T[3]), "s"(r)
      : "memory", "vcc", "scc", "v164", "s80", "s81", "s82", "s83", "s84", "s85", "s88", "s89", "s90", "s92", "s93", "s91", "s96");
}
template <int STRIDE, int S, int MODE = 0, int K = 0, int N> __device__ __forceinline__ void gather(rsrc_t rws, unsigned off0, u32x4 T, f32x4 (&o)[N]) {
  unsigned off[N];
  u32x4 v[N];
#pragma unroll
  for (int j = 0; j < N; ++j) { off[j] = off0 >= OOB ? OOB : off0 + j * STRIDE; v[j] = T; }
  SCHED();
  poll<N, S, MODE, K>(rws, off, T, v);
  SCHED();
#pragma unroll
  for (int j = 0; j < N; ++j) o[j] = asf4(v[j] ^ T);
}
__device__ __forceinline__ void turn(const u32x4* w, u32x8* o) {
#pragma unroll
  for (int k = 0; k < 4; ++k)
#pragma unroll
    for (int r = 0; r < 8; ++r) { o[2 * k][r] = plo(w[2 * r][k], w[2 * r + 1][k]); o[2 * k + 1][r] = phi(w[2 * r][k], w[2 * r + 1][k]); }
#pragma unroll
  for (int i = 0; i < 8; ++i) asm volatile("" : "+v"(o[i]));
}
constexpr int krow2(int t) { return 32 * (t >> 2) + (t & 3); }
__device__ __forceinline__ void turn2(const u32x4* w, u32x8* o) {
#pragma unroll
  for (int k = 0; k < 4; ++k)
#pragma unroll
    for (int r = 0; r < 4; ++r) { o[k][2 * r] = plo(w[2 * r][k], w[2 * r + 1][k]); o[k][2 * r + 1] = phi(w[2 * r][k], w[2 * r + 1][k]); }
#pragma unroll
  for (int i = 0; i < 4; ++i) asm volatile("" : "+v"(o[i]));
}
template <int S = 2> __device__ __forceinline__ void lds_wait(unsigned* w, unsigned want) {
  POLL_LOOP {
    if (__hip_atomic_load(w, __ATOMIC_RELAXED, __HIP_MEMORY_SCOPE_WORKGROUP) >= want) break;
    if (S) __builtin_amdgcn_s_sleep(S);
  }
  asm volatile("" ::: "memory");
}
__device__ __forceinline__ void lds_post(unsigned* w, int lane, unsigned n = 1u) {
  asm volatile("s_waitcnt lgkmcnt(0)" ::: "memory");
  if (lane == 0) __hip_atomic_fetch_add(w, n, __ATOMIC_RELAXED, __HIP_MEMORY_SCOPE_WORKGROUP);
}

__device__ __forceinline__ void raise(unsigned* w) { asm volatile("" ::: "memory"); __hip_atomic_store(w, 1u, __ATOMIC_RELAXED, __HIP_MEMORY_SCOPE_WORKGROUP); }
template <int S> __device__ __forceinline__ void await(unsigned* w, int n) {
  POLL_LOOP {
    if (__builtin_popcountll(__builtin_amdgcn_ballot_w64(__hip_atomic_load(w, __ATOMIC_RELAXED, __HIP_MEMORY_SCOPE_WORKGROUP) != 0u)) >= n) break;
    if (S) __builtin_amdgcn_s_sleep(S);
  }
  asm volatile("" ::: "memory");
}
__device__ __forceinline__ float score(const u32x4* k, const u32x4* q) {
  float d0 = 0.f, d1 = 0.f;
#pragma unroll
  for (int j = 0; j < 3; ++j) {
    d0 = dot2(k[j][0], q[j][0], d0); d1 = dot2(k[j][1], q[j][1], d1);
    d0 = dot2(k[j][2], q[j][2], d0); d1 = dot2(k[j][3], q[j][3], d1);
  }
  return bfr(bfr(sum8(d0 + d1)) * 0.07216878364870322f);
}
__device__ __forceinline__ void pv8(u32x4 v, unsigned pl, float* acc) {
  const unsigned ph = pl << 16;
#pragma unroll
  for (int k = 0; k < 4; ++k) { acc[2 * k] = dot2(v[k], pl, acc[2 * k]); acc[2 * k + 1] = dot2(v[k], ph, acc[2 * k + 1]); }
}
__device__ __forceinline__ float fold8(const float* acc, int lane) {
  return tr8(tr16(tr32(acc[0], acc[4]), tr32(acc[2], acc[6])), tr16(tr32(acc[1], acc[5]), tr32(acc[3], acc[7])), lane & 8);
}

struct __attribute__((aligned(16))) Lds {
  unsigned dump[64];
  float ored[NSW][DH];
  float pred[NSW + 1][64];
  float fred[10][128];
  float cred[10][128];
  unsigned short qs[DH];
  unsigned short act[72];
  float ml[NSW][2];
  unsigned tag[4];
  unsigned cnt[4];
  unsigned flag[24];
  unsigned hsum[2][32];
  unsigned near;
  unsigned pad[1];
  u32x4 wst[2][512][8];
};

struct Plan { unsigned w[3 * NWG + 2]; };
int job_of(int h, int s) {
  const int e = (s - h - 1) & 7;
  return h >= NF ? h * 8 + s : e == 7 ? h : e >= 2 ? NF + s * 15 + (h >> 3) * 5 + e - 2 : NF + NP + 2 * h + e;
}
#define UNDEF8(w) asm volatile("" : "=v"(w[0]), "=v"(w[1]), "=v"(w[2]), "=v"(w[3]), "=v"(w[4]), "=v"(w[5]), "=v"(w[6]), "=v"(w[7]))
#define LOFF(x) (unsigned)((const char*)&(x) - (const char*)&L)
#define LAT(T, off) ((T*)((char*)&L + (off)))
typedef const unsigned short* cptr;
typedef const __attribute__((address_space(1))) unsigned short* gptr;
typedef const __attribute__((address_space(4))) unsigned long long* kargs_t;
#define KARGS kargs_t ka = (kargs_t)__builtin_amdgcn_kernarg_segment_ptr(); asm volatile("" : "+s"(ka))
#define ARG(i) ((gptr)ka[i])
__global__ __launch_bounds__(NTHR, 3) void k_fused(cptr, cptr, cptr, cptr, cptr, cptr, cptr, cptr, unsigned short*, unsigned char*, Plan) {
  __shared__ Lds L;
  asm volatile(".Lcode_begin:\n\ts_mul_i32 vcc_lo, %1, 12\n\ts_load_dwordx4 s[96:99], %0, 0x8\n\ts_load_dwordx4 s[92:95], %0, vcc_lo offset:0x50\n\ts_load_dwordx2 s[100:101], %0, 0x18"
               ::"s"(__builtin_amdgcn_kernarg_segment_ptr()), "s"(blockIdx.x) : "vcc", "s92", "s93", "s94", "s95", "s96", "s97", "s98", "s99", "s100", "s101");
  SCHED();
  const unsigned tid0 = __builtin_amdgcn_readfirstlane(threadIdx.x);
  int wave = tid0 >> 6;
  {
    unsigned t;
    asm volatile("s_or_b32 s88, %1, %2\n\ts_cmp_lt_u32 s88, %3\n\ts_cbranch_scc0 1f\n\ts_getpc_b64 s[88:89]\n\ts_mov_b32 m0, %4\n\tv_lshlrev_b32 %0, 7, %5\n\ts_andn2_b32 s88, s88, 0xff\n\ts_and_b32 s89, s89, 0xffff\n\t"
                 "s_mov_b32 s90, .Lcode_end-.Lcode_begin\n\ts_max_u32 s90, s90, .Lcode_end2-.Lcode_begin\n\ts_max_u32 s90, s90, .Lcode_end3-.Lcode_begin\n\ts_max_u32 s90, s90, .Lcode_end4-.Lcode_begin\n\ts_mov_b32 s91, 0x20000\n\t"
                 "s_movk_i32 s87, 0x2000\n\tbuffer_load_dword %0, s[88:91], 0 offen lds\n\tbuffer_load_dword %0, s[88:91], s87 offen lds\n\ts_movk_i32 s87, 0x4000\n\t"
                 "buffer_load_dword %0, s[88:91], s87 offen lds\n1:"
                 : "=&v"(t) : "s"(blockIdx.x), "s"(tid0), "n"(CPF), "n"(0), "v"(threadIdx.x) : "memory", "scc", "s87", "s88", "s89", "s90", "s91");
    SCHED();
  }
  u32x4 first[6];
  gptr kc0, vc0, wq;
  unsigned kvo, plan, vq;
  unsigned long long tk0;
  {
    const bool st = wave < NSW;
    unsigned so[6], idx = (threadIdx.x >> 3) & 63u, c16 = (threadIdx.x & 7u) * 16u, size = st ? 32u * SK * DH * 2u : D * 6144u * 2u;
#pragma unroll
    for (int i = 1; i < 6; ++i) so[i] = st ? (i % 3) * 128 + (i / 3) * 24576 : i * 12288;
    asm volatile("" : "+s"(so[1]), "+s"(so[2]), "+s"(so[3]), "+s"(so[4]), "+s"(so[5]), "+s"(size));
    asm volatile("" : "+v"(idx), "+v"(c16));
    gptr p;
    asm volatile("s_cmp_lt_u32 %0, 8\n\ts_cbranch_scc1 1f\n\ts_setprio 3\n1:" ::"s"(wave) : "scc");
    asm volatile("s_cmp_lt_u32 %3, 8\n\ts_waitcnt lgkmcnt(0)\n\ts_cselect_b32 s93, s92, s93\n\tv_add_u32 %1, s93, %4\n\ts_cselect_b64 %0, s[96:97], s[100:101]\n\t"
                 "s_cselect_b32 s95, 0, s94\n\ts_bfe_u32 s95, s95, 0x4000a\n\ts_mul_i32 s95, s95, 0x3000\n\ts_max_u32 s95, s95, 0x180\n\tv_mad_u32_u24 %1, %2, s95, %1"
                 : "=&s"(p), "=&v"(vq) : "v"(idx), "s"(wave), "v"(c16) : "scc", "s93", "s95");
    const rsrc_t r = mk(p, size);
#pragma unroll
    for (int i = 0; i < 6; ++i) {
      if (i == 0) { SCHED(); tk0 = __builtin_amdgcn_s_memrealtime(); SCHED(); }
      first[i] = ld128<AUX_NT>(r, vq, i ? so[i] : 0u);
    }
    SCHED();
    asm volatile("s_mov_b64 %0, s[96:97]\n\ts_mov_b64 %1, s[98:99]\n\ts_mov_b32 %2, s92\n\ts_mov_b32 %3, s94" : "=s"(kc0), "=s"(vc0), "=s"(kvo), "=s"(plan));
    wq = p;
  }
  int lane = threadIdx.x & 63, b = blockIdx.x;
  asm volatile("" : "+v"(lane));
  asm volatile("" : "+s"(wave), "+s"(b), "+s"(plan));
  const int h = b >> 3, s = b & 7;
  const int g = lane >> 3, c = lane & 7, tk = plan >> 16;
  const bool stream = wave < NSW, isF = tk < NF;
  const int a = wave - NSW;
  u32x2 e1 = {0u, 0u};
  u32x4 T = {0u, 0u, 0u, 0u};
  u32x4 wt[8];

  if (__builtin_expect(stream, 0)) {
    if (wave < 2) {
      const unsigned t = wave * 64 + lane, i = t - 12u;
      if (t < 94u) L.cnt[t] = i < 16u ? i >= 10u : i < 80u && ((i - 16u) & 31u) < 20u ? ~0u : 0u;
    }
    const int nk = plan & 1023;
    const unsigned voff = vq - kvo;
    const rsrc_t rk = mk(kc0 + kvo / 2, nk * 384), rv = mk(vc0 + kvo / 2, nk * 384);
    u32x4 kb[27], vb[27];
    unsigned due, tau, tau8;
#pragma unroll
    for (int i = 0; i < 27; ++i) {
      if (i == KW) bar();
      if (i == KW) {
        const bool nine = 512 + 8 * wave < nk;
        tau = isF ? 0u : tk < NF + NP ? (nine ? 16 * (SP - SA) / 53 : 16 * (SP - SA) / 47) : nine ? 16 * (SE - SA) / 53 : 16 * (SE - SA) / 47;
        tau8 = nine ? tau : 0u;
        due = ((unsigned)tk0 << 4) + (isF ? -65536 : 16 * SA) + (KW - 1) * tau;
        asm volatile("s_memrealtime s[92:93]" : "+s"(tau), "+s"(tau8) :: "s92", "s93");
      }
      if (i >= KW) GATE(KW, i < 24 ? tau : tau8, i);
      kb[i] = i < 6 ? first[i] : ld128<AUX_NT>(rk, voff + (i % 3) * 128, (i / 3) * 24576);
      SCHED();
    }
#define LISTLOAD(n) vb[n] = ld128<AUX_NT>(rv, voff + ((n) % 3) * 128, (8 - (n) / 3) * 24576)
#pragma unroll
    for (int n = 0; n < VW; ++n) {
      GATE(KW, n < 3 ? tau8 : tau, 27 + n);
      LISTLOAD(n);
      SCHED();
    }
    lds_wait(&L.cnt[1], 1);
    u32x4 qv[3];
#pragma unroll
    for (int j = 0; j < 3; ++j) qv[j] = *(const u32x4*)&L.qs[j * 64 + c * 8];
    float sc[9];
#pragma unroll
    for (int t = 0; t < 9; ++t) {
      sc[t] = score(kb + 3 * t, qv);
      if (t & 1) {
        SCHED();
#pragma unroll
        for (int n = VW + VN * (t - 1) / 8; n < VW + VN * (t + 1) / 8; ++n) LISTLOAD(n);
        SCHED();
      }
    }
    due += VN * tau;
    asm volatile("s_memrealtime s[92:93]" : "+s"(due) :: "s92", "s93");
    constexpr int V1 = VW + VN;
#pragma unroll
    for (int t = 1; t < 9; ++t) if (64 * t + 8 * wave >= nk) sc[t] = -__builtin_inff();
    float m = sc[0];
#pragma unroll
    for (int t = 1; t < 9; ++t) m = fmaxf(m, sc[t]);
    m = maxg(m);
    float lsum = 0.f;
    unsigned pp[9];
#pragma unroll
    for (int t = 0; t < 9; ++t) { const float p = __builtin_amdgcn_exp2f((sc[t] - m) * L2E); lsum += p; pp[t] = pk(p, 0.f); }
    lsum = sumg(lsum);
    if (lane == 0) *(f32x2*)L.ml[wave] = f32x2{m, lsum};
    lds_post(&L.cnt[0], lane);
    float acc[24];
    unsigned odst = LOFF(L.ored[wave][8 * c + g]);
    KARGS;
    const rsrc_t rw = mk(isF ? ARG(7) : ARG(4), isF ? FF * D * 2 : tk < NF + NP ? 6144 * D * 2 : 0);
    unsigned wvo = (unsigned)(isF ? klane(lane) + (lane & 8) : (lane >> 4) + (lane & 8)) * 1280u + c * 16u;
    int ws0 = isF ? 64 * tk * 1280 + wave * 128 : ((tk - NF) / 10 * 512 + 64 * wave) * 1280 + (tk - NF) % 10 * 128, ws1 = isF ? 1280 : 4 * 1280, ws2 = isF ? 2 * 1280 : 16 * 1280;
    const rsrc_t rws = mk(ARG(9), WS_END);
    unsigned oo = WS_O - 1u + (unsigned)g * O_ROW + orow((unsigned)((tk - NF) / 10 * 512 + 64 * wave + 4 * c) * 4u);
    asm volatile("" : "+v"(odst), "+v"(wvo), "+v"(oo));
    asm volatile("" : "+s"(ws0), "+s"(ws1), "+s"(ws2));
#define WLOAD(t) wt[t] = ld128<AUX_NT>(rw, wvo, ws0 + ((t) & 1) * ws1 + (((t) >> 1) & 1) * ws2 + ((t) >> 2) * 32 * 1280)
#pragma unroll
    for (int e = 0; e < 24; ++e) acc[e] = 0.f;
#pragma unroll
    for (int n = 0; n < 27; ++n) {
      if (n + VP >= V1 && n + VP < 27) { GATE(VP, tau, 27 + n + VP); LISTLOAD(n + VP); }
      SCHED();
      pv8(vb[n], pp[8 - n / 3], acc + 8 * (n % 3));
      if (n + WW >= 27 && n < 26) { SCHED(); WLOAD(n + WW - 27); SCHED(); }
      if (n >= 24) *LAT(float, odst + (n % 3) * 256) = fold8(acc + 8 * (n % 3), lane);
      SCHED();
    }
    if (tk >= NF + NP) {
      bar();
      LEAVE();
    } else {
      raise(&L.flag[wave]);
    }
    if (isF) {
#pragma unroll
      for (int t = WW - 1; t < 8; ++t) { VMWAIT(W2 - 1); WLOAD(t); SCHED(); }
    } else if (tk < NF + NP) {
      T = *(const u32x4*)L.tag;
      lds_wait(&L.near, 1);
      oo += L.near;
      unsigned off[2] = {oo, oo + 128u}, pdst = LOFF(L.pred[g == 0 ? wave : NSW][8 * c]);
      int idx = smf_idx(lane & 1);
      asm volatile("" : "+v"(pdst), "+v"(idx), "+v"(off[0]), "+v"(off[1]), "+v"(T));
      f32x4 d[4];
      zero(d);
      SCHED();
#pragma unroll
      for (int t = WW - 1; t < 8; ++t) {
        VMWAIT(W2 - 1);
        while ((int)((unsigned)__builtin_amdgcn_s_memrealtime() - ((unsigned)tk0 + SW - (7 - t) * TW)) < 0) __builtin_amdgcn_s_sleep(1);
        WLOAD(t);
        SCHED();
      }
      VMWAIT(0);
      u32x4 v[2] = {ld128<AUX_SC1>(rws, off[0], 0), ld128<AUX_SC1>(rws, off[1], 0)};
      SCHED();
      u32x8 wp[4];
      turn2(wt, wp);
      SCHED();
      poll<2, OS + 256 * OL + 65536 * OK, 5, 0>(rws, off, T, v);
      SCHED();
      float o8[8];
#pragma unroll
      for (int i = 0; i < 8; ++i) o8[i] = asf(v[i >> 2][i & 3] ^ T[i & 3]);
      const float o1 = fold8(o8, lane);
      const unsigned op = pk(dpp<0xA0>(o1), dpp<0xF5>(o1));
      const u32x4 av = {op, shl<4>(op), shl<8>(op), shl<12>(op)};
#pragma unroll
      for (int k = 0; k < 4; ++k) d[k] = smf4(av, wp[k], d[k], idx);
      SCHED();
#pragma unroll
      for (int k = 0; k < 4; k += 2) {
        f32x4 r = {d[k][0] + dpp<ROR8>(d[k][2]), 0.f, d[k + 1][0] + dpp<ROR8>(d[k + 1][2]), 0.f};
        asm volatile("" : "+v"(r[0]), "+v"(r[2]));
        r[1] = d[k][1] + dpp<ROR8>(d[k][3]); r[3] = d[k + 1][1] + dpp<ROR8>(d[k + 1][3]);
        *LAT(f32x4, pdst + 8 * k) = r;
      }
      bar();
      LEAVE();
      asm volatile(".Lcode_end4:");
    }
  } else if (__builtin_expect(isF && a < 2, 0)) {
    KARGS;
    const rsrc_t rw = mk((a ? ARG(6) : ARG(5)) + 64 * h, D * FF * 2 - 128 * h);
    const unsigned vo = (unsigned)g * 3072u + c * 16u;
    unsigned so = 0u, dst = LOFF(L.wst[a]) + (unsigned)(size_t)&L;
#define DMA() asm volatile("s_mov_b32 m0, %1\n\ts_add_u32 %1, %1, 0x400\n\tbuffer_load_dwordx4 %2, %3, %0 offen nt lds\n\ts_add_u32 %0, %0, 0x6000" : "+s"(so), "+s"(dst) : "v"(vo), "s"(rw) : "memory", "scc")
    bar();
#pragma nounroll
    for (int i = 0; i < 64; ++i) { VMWAIT(DW - 1); DMA(); }
    asm volatile(".Lcode_end3:");
    UNDEF8(wt);
  } else {
    const int nl = (plan >> 10) & 15, qpart = (plan >> 14) & 3;
    const bool big = nl > 8, qjob = nl != 0;
    constexpr int NS = 8;
    const rsrc_t rq = mk(wq, D * 6144 * 2);
    u32x4 wb[NB];
#pragma unroll
    for (int i = 0; i < NS; ++i) wb[i] = i < 6 ? first[i] : ld128<AUX_NT>(rq, nl > 6 ? vq : OOB, i * 12288);
    if (big) {
#pragma unroll
      for (int i = NS; i < NB; ++i) wb[i] = ld128<AUX_NT>(rq, vq, i * 12288);
    }
    const unsigned row = vq / 12288u, qline = __builtin_amdgcn_readfirstlane((vq - row * 12288u) >> 7), xo = qjob ? row * 2u : OOB;
    SCHED();
    KARGS;
    const rsrc_t rws = mk(ARG(9), WS_END), rx = mk(ARG(0), D * 2);
    const u32x4 xs4 = ld128<0>(rx, (unsigned)lane * 16u, 0);
    const unsigned xs1 = ld32<0>(rx, 1024u + (unsigned)lane * 4u, 0);
    const u32x4 xr0 = ld128<0>(rx, xo, 0), xr4 = ld128<0>(rx, xo + 16u, 0);
    const u32x2 ep = ld64<AUX_SC1>(rws, WS_EPOCH, 0);
    unsigned pf[3] = {0u, 0u, 0u};
    if (a == 3) {
      pf[0] = ld32<0>(mk(ARG(8), D * 2), 0, 0);
      const unsigned kvn = (plan & 1023u) * 384u, po = lane == 0 ? 0u : lane == 1 ? kvn - 128u : OOB;
      pf[1] = ld32<AUX_NT>(mk(vc0 + kvo / 2, kvn), po, 0);
      pf[2] = ld32<AUX_NT>(mk(kc0 + kvo / 2, kvn), lane == 1 ? po : OOB, 0);
    }
    bar();
    f32x4 qp = {0.f, 0.f, 0.f, 0.f};
    if (qjob) {
      const unsigned xs[5] = {xs4[0], xs4[1], xs4[2], xs4[3], xs1}, xr[7] = {xr0[0], xr0[1], xr0[2], xr0[3], xr4[0], xr4[1], xr4[2]};
      float sm = 0.f;
#pragma unroll
      for (int i = 0; i < 5; ++i) sm += lo16(xs[i]) + hi16(xs[i]);
      const float mean = sum64(sm) * (1.f / D);
      float sq = 0.f;
#pragma unroll
      for (int i = 0; i < 5; ++i) { const float d0 = lo16(xs[i]) - mean, d1 = hi16(xs[i]) - mean; sq += d0 * d0 + d1 * d1; }
      const float mb = bfr(mean), rden = rstd(bfr(sum64(sq) * (1.f / D)));
      float qa[8];
#pragma unroll
      for (int k = 0; k < 8; ++k) qa[k] = 0.f;
      auto rows = [&](int i) {
        const unsigned xp = pk(bfr(lo16(xr[i]) - mb) * rden, bfr(hi16(xr[i]) - mb) * rden);
#pragma unroll
        for (int k = 0; k < 4; ++k) {
          qa[2 * k] = dot2(plo(wb[2 * i][k], wb[2 * i + 1][k]), xp, qa[2 * k]);
          qa[2 * k + 1] = dot2(phi(wb[2 * i][k], wb[2 * i + 1][k]), xp, qa[2 * k + 1]);
        }
      };
#pragma unroll
      for (int i = 0; i < NS / 2; ++i) rows(i);
      if (big) {
#pragma unroll
        for (int i = NS / 2; i < NB / 2; ++i) rows(i);
      }
#pragma unroll
      for (int i = 0; i < 4; ++i) { float t = tr32(qa[i], qa[i + 4]); t = tr16(t, t); qp[i] = t + dpp<ROR8>(t); }
    }
    if ((lane & 24) == 0) *(f32x4*)&L.pred[a][c * 8 + (lane >> 5) * 4] = qp;
    lds_post(&L.cnt[3], lane);
    const unsigned long long en = (((unsigned long long)ep[1] << 32) | ep[0]) + 1ull;
    e1 = u32x2{(unsigned)en, (unsigned)(en >> 32)};
    T = make_tag(e1);
    if (a == 0 && qjob) {
      lds_wait(&L.cnt[3], 4);
      const float* src = &L.pred[0][4 * (lane & 15)];
      qp = (*(const f32x4*)src + *(const f32x4*)(src + 64)) + (*(const f32x4*)(src + 128) + *(const f32x4*)(src + 192));
      const unsigned ex = asu(fmaxf(fmaxf(fabsf(qp[0]), fabsf(qp[1])), fmaxf(fabsf(qp[2]), fabsf(qp[3])))) >> 23;
      u32x4 qo;
#pragma unroll
      for (int i = 0; i < 4; ++i) qo[i] = ((unsigned)min((int)__builtin_rintf(__builtin_ldexpf(qp[i], 145 - (int)ex)), 524287) << 12) | (((ex >> (2 * i)) & 3u) << 10) | T[i];
      st128(qo, rws, lane < 16 ? WS_Q + (unsigned)qline * 1024u + qpart * 256u + lane * 16u : OOB);
    } else if (a == 2) {
      unsigned off[3];
      u32x4 v[3];
#pragma unroll
      for (int j = 0; j < 3; ++j) {
        const int ql = 3 * h + j;
        off[j] = lane < (ql >= 73 || (ql & 7) == 3 ? 48 : 32) ? WS_Q + (unsigned)ql * 1024u + lane * 16u : OOB;
        v[j] = T;
      }
      while (__builtin_amdgcn_s_memrealtime() < tk0 + QT) __builtin_amdgcn_s_sleep(4);
      SCHED();
      poll<3, QS, 0, 0>(rws, off, T, v);
      SCHED();
#pragma unroll
      for (int j = 0; j < 3; ++j) {
        const int ex = (int)(((v[j][0] >> 10) & 3u) | ((v[j][1] >> 8) & 12u) | ((v[j][2] >> 6) & 48u) | ((v[j][3] >> 4) & 192u)) - 145;
        f32x4 qf;
#pragma unroll
        for (int i = 0; i < 4; ++i) { float t = __builtin_ldexpf((float)((int)v[j][i] >> 12), ex); t = tr16(t, t); qf[i] = tr32(t, t); }
        if (lane < 16) *(u32x2*)&L.qs[j * 64 + lane * 4] = u32x2{pk(qf[0], qf[1]), pk(qf[2], qf[3])};
      }
      if (lane == 0) *(u32x4*)L.tag = T;
      lds_post(&L.cnt[1], lane);
    } else if (a == 3) {
      st128(T, rws, (s == (h & 7) && lane < 32) ? WS_PR + (unsigned)h * 256u + (lane & 15) * 16u + (lane >> 4) * TWIN : OOB);
      asm volatile("" ::"v"(pf[0]), "v"(pf[1]), "v"(pf[2]));
    }
    asm volatile(".Lcode_end2:");
    UNDEF8(wt);
  }

  const int wave0 = wave, tk1 = tk;
  {
    int lane = threadIdx.x & 63, wave = wave0, b = blockIdx.x, tk = tk1;
    asm volatile("" : "+v"(lane));
    asm volatile("" : "+s"(wave), "+s"(b), "+s"(tk));
    const int g = lane >> 3, c = lane & 7, h = b >> 3, s = b & 7, a = wave - NSW;
    const bool stream = wave < NSW;
    KARGS;
    const gptr w1 = ARG(5), w3 = ARG(6), w2 = ARG(7), out = ARG(8), ws = ARG(9);
    const rsrc_t rws = mk(ws, WS_END), rx = mk(ARG(0), D * 2);
    auto near = [&]() { lds_wait(&L.near, 1); return (unsigned)__builtin_amdgcn_readfirstlane(L.near) - 1u; };
    float fw = 0.f;
    if (a == 3) {
      const unsigned half = (__builtin_amdgcn_s_getreg(20 | (3 << 11)) >> 2) & 1u;
      while (__builtin_amdgcn_s_memrealtime() < tk0 + PT) __builtin_amdgcn_s_sleep(8);
      unsigned tt[2] = {~0u, ~0u};
#pragma unroll
      for (int r = 0; r < 4; ++r) {
        unsigned long long t0, t1;
        unsigned x;
        asm volatile("s_memtime %0\n\ts_waitcnt lgkmcnt(0)\n\ts_load_dword %2, %3, %4\n\ts_waitcnt lgkmcnt(0)\n\ts_memtime %1\n\ts_waitcnt lgkmcnt(0)"
                     : "=&s"(t0), "=&s"(t1), "=&s"(x) : "s"(ws + (WS_PR + h * 256) / 2), "n"((r >> 1) * 128 + (r & 1) * TWIN) : "memory");
        tt[r & 1] = min(tt[r & 1], (unsigned)(t1 - t0));
      }
      const int dt = max(-256, min(256, (int)(tt[0] - tt[1])));
      lds_wait(&L.cnt[0], NSW);
      const f32x2 mlw = *(const f32x2*)L.ml[c];
      const float mwg = asf(asu(max8(mlw[0])) & ~0x3FFu);
      const float lwg = sum8(mlw[1] * __builtin_amdgcn_exp2f((mlw[0] - mwg) * L2E));
      const u32x4 own = tagged(f32x4{mwg, lwg, (float)(half ? -dt : dt), 0.f}, T);
      st128(own, rws, (lane == 0 ? WS_ML + (unsigned)b * 128u : OOB));
      f32x4 mo[1];
      gather<0, MS>(rws, ((lane < 8 && lane != s) ? WS_ML + (unsigned)(h * 8 + lane) * 128u : OOB), T, mo);
      if (lane == s) mo[0] = asf4(own ^ T);
      const unsigned pol = sum8(mo[0][2]) > 0.f;
      if (lane == 0) __hip_atomic_store(&L.near, 1u + ((pol ^ half) & 1u) * TWIN, __ATOMIC_RELAXED, __HIP_MEMORY_SCOPE_WORKGROUP);
      const float mh = max8(mo[0][0]);
      fw = __builtin_amdgcn_exp2f((mlw[0] - mh) * L2E) * __builtin_amdgcn_rcpf(sum8(mo[0][1] * __builtin_amdgcn_exp2f((mo[0][0] - mh) * L2E)));
    }
    if (a == 3) {
      const int dq = min(lane, 47);
      unsigned src = LOFF(L.ored[0][4 * dq]);
      unsigned so = (lane < 48 ? WS_O + (unsigned)s * O_ROW + orow((unsigned)h * (DH * 4) + lane * 16u) : OOB);
      float f[NSW];
#pragma unroll
      for (int w = 0; w < NSW; ++w) { f[w] = rdl(fw, w); asm volatile("" : "+s"(f[w])); }
      asm volatile("" : "+v"(src), "+v"(so));
      f32x4 t[NSW];
      if (tk >= NF + NP) {
        bar();
#pragma unroll
        for (int w = 0; w < NSW; ++w) t[w] = *LAT(const f32x4, src + w * DH * 4);
      } else {
        await<2>(&L.flag[c], 64);
#pragma unroll
        for (int w = 0; w < NSW; ++w) t[w] = *LAT(const f32x4, src + w * DH * 4);
      }
#pragma unroll
      for (int w = 0; w < NSW; ++w) t[w] *= f[w];
      const f32x4 o = ((t[0] + t[1]) + (t[2] + t[3])) + ((t[4] + t[5]) + (t[6] + t[7]));
      const u32x4 ot = tagged(o, T);
      st128(ot, rws, so);
      st128(ot, rws, so + TWIN);
    }
    if (tk < NF) {
      if (wave < 10) {
        u32x4 wb[16];
        unsigned xv;
        {
          const rsrc_t rw2 = mk(w2, FF * D * 2);
          xv = __builtin_amdgcn_raw_buffer_load_b16(rx, (unsigned)(64 * wave + hrow(lane)) * 2u, 0, 0);
          if (!stream) {
            const gptr pw = ((lane & 8) ? w3 : w1) + (64 * wave + hrow(klane(lane))) * FF + 64 * tk + 8 * c;
#pragma unroll
            for (int t = 0; t < 16; ++t) {
              VMWAIT(DW - 1);
              wb[t] = __builtin_nontemporal_load((const __attribute__((address_space(1))) u32x4*)(pw + hrow(krow(t)) * FF));
              SCHED();
            }
            VMWAIT(16);
            lds_post(&L.cnt[3], lane, 256u);
            await<8>(&L.flag[c], 8);
#pragma unroll
            for (int t = 0; t < 8; ++t) {
              VMWAIT(WW - 1);
              wt[t] = ld128<AUX_NT>(rw2, (unsigned)(klane(lane) + (lane & 8)) * 1280u + c * 16u, (64 * tk + krow2(t)) * 1280 + wave * 128);
              SCHED();
            }
          }
          if (stream) {
            lds_wait(&L.cnt[3], 512);
#pragma unroll
            for (int t = 0; t < 16; ++t) wb[t] = L.wst[(lane >> 3) & 1][64 * wave + hrow(klane(lane)) + hrow(krow(t))][c];
          }
        }
        u32x8 b1[8], b2[4];
        turn(wb, b1); turn2(wt, b2);
        SCHED();
        T = *(const u32x4*)L.tag;
        int idx = smf_idx(lane & 8), idx2 = smf_idx(lane & 1);
        unsigned fdst = LOFF(L.fred[wave][(lane >> 5) * 64 + 2 * c + (g & 1)]);
        {
          const u32x4 ones = {0x3F803F80u, 0x3F803F80u, 0x3F803F80u, 0x3F803F80u};
#pragma unroll
          for (int i = 0; i < 4; ++i) *LAT(float, fdst + (LOFF(L.cred) - LOFF(L.fred)) + i * 64) = tr32(smf(ones, b1[i], idx), smf(ones, b1[i + 4], idx));
        }
        raise(&L.flag[8 + wave]);
        f32x4 pv[3];
        float xf = lo16(xv);
        if (lane & 16) fdst = LOFF(L.pred);
        unsigned hdst = (lane == 0 || lane == 40) ? LOFF(L.hsum[0][2 * wave + (g & 1)]) : LOFF(L.pred), asrc = LOFF(L.act[(lane & 48) + 4 * (lane & 2)]);
        unsigned as0 = lane & 8 ? LOFF(L.act[64]) : asrc, as1 = lane & 8 ? asrc : LOFF(L.act[64]);
        unsigned so = ((lane & 8) == 0 ? WS_P3 + prow(tk) + (lane & 16) * (TWIN / 16) + (unsigned)wave * 256u + (lane >> 5) * 128u + c * 16u : OOB);
        asm volatile("" : "+v"(fdst), "+v"(hdst), "+v"(xf), "+v"(as0), "+v"(as1), "+v"(so), "+v"(idx), "+v"(idx2));
        f32x4 d[8], e[2];
        zero(d);
        u32x4 ones = {0x3F803F80u, 0x3F803F80u, 0x3F803F80u, 0x3F803F80u};
        asm volatile("" : "+v"(ones));
        gather<32768, PS + 256 * PLS + 65536 * PK>(rws, WS_P2 + near() + prow(lane >> 4) + (unsigned)wave * 256u + (unsigned)(lane & 15) * 16u, T, pv);
        {
          const f32x4 ps = (pv[0] + pv[1]) + pv[2];
          const float at = tr16(tr32(ps[0], ps[2]), tr32(ps[1], ps[3]));
          const float hv = bfr(xf + bfr(at));
          const unsigned hp = phi(asu(hv), asu(dpp<XOR1>(hv)));
          const u32x4 a0 = {hp, shl<2>(hp), shl<4>(hp), shl<6>(hp)};
          const float s1 = mfma(a0, ones), s2 = mfma(a0, a0);
          SCHED();
#pragma unroll
          for (int i = 0; i < 2; ++i) d[i] = smf4(a0, b1[(i >> 1) + 4 * (i & 1)], d[i], idx);
          SCHED();
          *LAT(float, hdst) = s1; *LAT(float, hdst + 128) = s2;
          SCHED();
#pragma unroll
          for (int i = 2; i < 8; ++i) d[i] = smf4(a0, b1[(i >> 1) + 4 * (i & 1)], d[i], idx);
#pragma unroll
          for (int i = 0; i < 4; ++i) *LAT(float, fdst + i * 64) = tr32(d[2 * i][0], d[2 * i + 1][0]);
        }
        bar();
        zero(e);
        bar();
        const u32x4 a0 = *LAT(const u32x4, as0), a1 = *LAT(const u32x4, as1);
        f32x4 d0 = smf4(a0, b2[0], e[0], idx2), d1 = smf4(a0, b2[1], e[1], idx2);
        SCHED();
        d0 = smf4(a1, b2[2], d0, idx2); d1 = smf4(a1, b2[3], d1, idx2);
        st128(tagged(f32x4{d0[0] + dpp<ROR8>(d0[2]), d0[1] + dpp<ROR8>(d0[3]), d1[0] + dpp<ROR8>(d1[2]), d1[1] + dpp<ROR8>(d1[3])}, T), rws, so);
        LEAVE();
      }
    } else if (a == 3 && tk >= NF + NP && tk < NF + NP + NR) {
      const int j = tk - NF - NP, col0 = 64 * (j >> 3) + 16 * (j & 3) + (j & 4) + 8 * (lane & 1), e = 2 * (lane >> 5) + ((lane >> 4) & 1);
      const unsigned xv = __builtin_amdgcn_raw_buffer_load_b16(rx, (unsigned)(col0 + e) * 2u, 0, 0);
      const unsigned fo = near() + prow(lane >> 1);
      auto fold = [&](f32x4 f) {
        const float r = tr16(tr32(f[0], f[2]), tr32(f[1], f[3]));
        return sum21(r + dpp<ROR8>(r));
      };
      f32x4 pv[1];
      {
        while (__builtin_amdgcn_s_memrealtime() < tk0 + G3) __builtin_amdgcn_s_sleep(2);
        const unsigned x = ld32<AUX_SC1>(rws, lane < 48 ? WS_P3 + fo + (unsigned)j * 32u : OOB, 0);
        asm volatile("" ::"v"(x));
      }
      while (__builtin_amdgcn_s_memrealtime() < tk0 + GR) __builtin_amdgcn_s_sleep(2);
      gather<0, HS>(rws, (lane < 24 ? WS_P2 + fo + (unsigned)col0 * 4u : OOB), T, pv);
      float hsel = bfr(lo16(xv) + bfr(fold(pv[0])));
      unsigned so = ((lane & 14) == 0 ? (unsigned)(col0 + e) * 2u : OOB), se = ((tk == NF + NP && lane == 0) ? WS_EPOCH : OOB);
      asm volatile("" : "+v"(hsel), "+v"(so), "+v"(se));
      gather<0, RS>(rws, (lane < 48 ? WS_P3 + fo + (unsigned)j * 32u + (lane & 1) * 16u : OOB), T, pv);
      __builtin_amdgcn_raw_buffer_store_b16((unsigned short)(pk(hsel + bfr(fold(pv[0])), 0.f) & 0xFFFFu), mk(out, D * 2), so, 0, AUX_SC1 | 1);
      st64(e1, rws, se);
      LEAVE();
    }
    if (a == 2) {
      if (tk < NF) {
        const int li = c * 16 + g * 2;
        if (lane < 4) *(unsigned*)&L.act[64 + 2 * lane] = 0u;
        await<2>(&L.flag[8 + (lane & 15)], 64);
        f32x2 cs = {0.f, 0.f};
#pragma unroll
        for (int j = 0; j < 10; ++j) cs += *(const f32x2*)&L.cred[j][li];
        unsigned src = LOFF(L.fred[0][li]), adst = LOFF(L.act[lane]);
        asm volatile("" : "+v"(cs), "+v"(src), "+v"(adst));
        unsigned* const hs = &L.hsum[0][lane];
        POLL_LOOP {
          if (__builtin_popcountll(__builtin_amdgcn_ballot_w64(__hip_atomic_load(hs, __ATOMIC_RELAXED, __HIP_MEMORY_SCOPE_WORKGROUP) != ~0u)) >= 28) break;
          __builtin_amdgcn_s_sleep(2);
        }
        unsigned sv, r0, r1, r2, r3, cap = SPIN_CAP;
#define LOOK(N, O) "ds_read_b32 %" #N ", %6\n\ts_waitcnt lgkmcnt(3)\n\tv_cmp_eq_u32 vcc, -1, %" #O "\n\tv_mov_b32 %0, %" #O "\n\ts_cbranch_vccz 2f\n\t"
        asm volatile("ds_read_b32 %1, %6\n\tds_read_b32 %2, %6\n\tds_read_b32 %3, %6\n"
                     "1:\n\t" LOOK(4, 1) LOOK(1, 2) LOOK(2, 3) LOOK(3, 4) "s_sub_u32 %5, %5, 1\n\ts_cbranch_scc0 1b\n"
                     "2:"
                     : "=&v"(sv), "=&v"(r0), "=&v"(r1), "=&v"(r2), "=&v"(r3), "+s"(cap) : "v"(LOFF(*hs) + (unsigned)(size_t)&L) : "vcc", "scc", "memory");
        SCHED();
        f32x2 t[10];
        asm volatile("s_barrier" ::: "memory");
        SCHED();
#pragma unroll
        for (int j = 0; j < 10; ++j) t[j] = *LAT(const f32x2, src + j * 512);
        SCHED();
        float s1 = sum8(asf(sv));
        s1 += dpp<ROR8>(s1);
        s1 = tr16(s1, s1);
        const auto s12 = __builtin_amdgcn_permlane32_swap(asu(s1), asu(s1), false, false);
        const float mean = asf(s12[0]) * (1.f / D), s2 = asf(s12[1]);
        float rden = rstd(bfr(fmaxf(s2 * (1.f / D) - mean * mean, 0.f)));
        cs *= -bfr(mean);
        asm volatile("" : "+v"(cs), "+v"(rden));
        SCHED();
        const f32x2 gu = ((((t[0] + t[1]) + (t[2] + t[3])) + ((t[4] + t[5]) + (t[6] + t[7]))) + ((t[8] + t[9]) + cs)) * rden;
        asm volatile("" ::"v"(r0), "v"(r1), "v"(r2), "v"(r3), "v"(gu));
        const unsigned gp = pk(gu[0], gu[1]);
        const float gt = lo16(gp);
        *LAT(unsigned short, adst) = (unsigned short)(pk(bfr(gt * __builtin_amdgcn_rcpf(1.f + __builtin_amdgcn_exp2f(-gt * L2E))) * hi16(gp), 0.f) & 0xFFFFu);
        bar();
        LEAVE();
      }
      if (tk >= NF && tk < NF + NP) {
        unsigned src = LOFF(L.pred[0][4 * (lane & 15)]);
        unsigned so = (lane < 32 ? WS_P2 + prow((tk - NF) / 10) + (lane & 16) * (TWIN / 16) + (unsigned)((tk - NF) % 10) * 256u + (lane & 15) * 16u : OOB);
        asm volatile("" : "+v"(src), "+v"(so));
        f32x4 t[NSW];
        bar();
#pragma unroll
        for (int w = 0; w < NSW; ++w) t[w] = *LAT(const f32x4, src + w * 256);
        st128(tagged(((t[0] + t[1]) + (t[2] + t[3])) + ((t[4] + t[5]) + (t[6] + t[7])), T), rws, so);
        LEAVE();
      }
    }
    if (!stream) {
      if (tk < NF) { if (a == 3) { bar(); bar(); } }
      else if (tk < NF + NP ? a != 2 : a != 3) bar();
      LEAVE();
    }
  }
  asm volatile(".Lcode_end:");
}

}

extern "C" void kernel_launch(void* const* d_in, const int* in_sizes, int n_in, void* d_out, int out_size, void* d_ws, size_t ws_size,
                              hipStream_t stream) {
  Plan plan;
  for (int b = 0; b < NWG; ++b) {
    const int h = b >> 3, s = b & 7, e = (s - h - 1) & 7;
    const bool big = h >= NF, slow = !big && (h & 7) == 3;
    const int kp = big ? 512 : slow ? KP2 : KP1, kn = big ? 512 : slow ? KN2 : KN1, kf = WIN - 2 * kn - 5 * kp, nk = e == 7 ? kf : e < 2 ? kn : kp;
    int k0 = 0;
    for (int j = 0; j != e; ++j) k0 += j < 2 ? kn : kp;
    const int qi = big ? 96 + s * 8 + h - NF : e < 2 ? 2 * h + e : 46 + (slow ? 21 + (h >> 3) : h - ((h + 4) >> 3)) * 5 + e;
    const bool qjob = (big || e != 7) && qi < 160;
    const int q2 = qi - 96, q3 = qi < 32 ? qi : (qi - 32) >> 1;
    const int qline = q2 >= 0 ? q2 + q2 / 7 + (q2 % 7 >= 3) : q3 < 9 ? q3 * 8 + 3 : 64 + q3, qpart = q2 >= 0 ? !big : qi < 32 ? 2 : qi & 1;
    const int nl = big ? NB : q2 >= 0 ? 20 - NB : qpart == 2 ? 8 : 6, row0 = (q2 >= 0 ? 32 * NB : 192) * qpart;
    plan.w[3 * b] = (unsigned)(h * SK + (SK - WIN) + k0) * (DH * 2);
    plan.w[3 * b + 1] = qjob ? (unsigned)(row0 * 12288 + qline * 128) : OOB;
    plan.w[3 * b + 2] = (unsigned)(nk | (qjob ? nl << 10 | qpart << 14 : 0) | job_of(h, s) << 16);
  }
  plan.w[3 * NWG] = plan.w[3 * NWG + 1] = 0u;
  hipLaunchKernelGGL(k_fused, dim3(NWG), dim3(NTHR), 0, stream, (const unsigned short*)d_in[0], (const unsigned short*)d_in[1],
                     (const unsigned short*)d_in[2], (const unsigned short*)d_in[3], (const unsigned short*)d_in[4],
                     (const unsigned short*)d_in[6], (const unsigned short*)d_in[7], (const unsigned short*)d_in[8],
                     (unsigned short*)d_out, (unsigned char*)d_ws, plan);
}
```

```cpp
#include <hip/hip_runtime.h>
#include <cstdint>

namespace {

constexpr int D = 640, DH = 192, SK = 32768, WIN = 4096, FF = 1536;
constexpr int NWG = 256, NSW = 8, NTHR = 768;
constexpr int KP1 = 544;
constexpr int KN1 = 576;
constexpr int KP2 = 560;
constexpr int KN2 = 576;
constexpr int NB = 12;
constexpr int KW = 6;
constexpr int VW = 6;
constexpr int VN = 4;
constexpr int VP = 6;
constexpr int WS = 3;
constexpr int WS2 = 2;
constexpr int D2 = 30;
constexpr int HP = 48;
constexpr int WW = 4;
constexpr int W2 = 5;
constexpr int DW = 8;
constexpr int SA = 80;
constexpr int SP = 1490;
constexpr int SE = 1570;
constexpr int SW = 1650;
constexpr int TW = 20;
constexpr int PT = 580;
constexpr int QT = 630;
constexpr int G3 = 1810;
constexpr int GR = 1880;
constexpr int QS = 10;
constexpr int MS = 4;
constexpr int OS = 1;
constexpr int OL = 8;
constexpr int OK = 32;
constexpr int PS = 0;
constexpr int PLS = 6;
constexpr int PK = 64;
constexpr int HS = 1;
constexpr int RS = 0;
constexpr int CPF = 16;
static_assert(KW == 6 && WIN - 2 * KN1 - 5 * KP1 >= 128 && WIN - 2 * KN2 - 5 * KP2 >= 128, "the first two key groups are never cut");

constexpr int NF = 24, NP = 120, NR = 80;
constexpr unsigned WS_EPOCH = 0;
constexpr unsigned TWIN = 8192;
__device__ __forceinline__ unsigned prow(unsigned r) { return (r >> 1) * 16384u + (r & 1) * 4096u; }
__device__ __forceinline__ unsigned orow(unsigned o) { return (o >> 13) * 16384u + (o & 8191u); }
constexpr unsigned WS_PR = 16384;
constexpr unsigned WS_P2 = 32768;
constexpr unsigned WS_P3 = WS_P2 + 6 * 16384;
constexpr unsigned O_ROW = 3 * 16384;
constexpr unsigned WS_O = WS_P3 + 12 * 16384;
constexpr unsigned WS_ML = WS_O + 8 * O_ROW;
constexpr unsigned WS_Q = WS_ML + 256 * 128;
constexpr unsigned WS_END = WS_Q + 96 * 1024;
constexpr unsigned OOB = 0x7F000000u;
constexpr int SPIN_CAP = 1 << 17;

typedef unsigned u32x2 __attribute__((ext_vector_type(2)));
typedef unsigned u32x4 __attribute__((ext_vector_type(4)));
typedef unsigned u32x8 __attribute__((ext_vector_type(8)));
typedef float f32x2 __attribute__((ext_vector_type(2)));
typedef float f32x4 __attribute__((ext_vector_type(4)));
typedef __bf16 bf16x2 __attribute__((ext_vector_type(2)));
typedef __bf16 bf16x8 __attribute__((ext_vector_type(8)));
typedef __bf16 bf16x16 __attribute__((ext_vector_type(16)));
typedef __amdgpu_buffer_rsrc_t rsrc_t;
constexpr int AUX_NT = 2, AUX_SC1 = 16;

template <class P> __device__ __forceinline__ rsrc_t mk(P p, unsigned bytes) { return __builtin_amdgcn_make_buffer_rsrc((void*)p, 0, bytes, 0x00020000); }
template <int AUX> __device__ __forceinline__ u32x4 ld128(rsrc_t r, unsigned vo, unsigned so) { return __builtin_amdgcn_raw_buffer_load_b128(r, vo, so, AUX); }
template <int AUX> __device__ __forceinline__ u32x2 ld64(rsrc_t r, unsigned vo, unsigned so) { return __builtin_amdgcn_raw_buffer_load_b64(r, vo, so, AUX); }
template <int AUX> __device__ __forceinline__ unsigned ld32(rsrc_t r, unsigned vo, unsigned so) { return __builtin_amdgcn_raw_buffer_load_b32(r, vo, so, AUX); }
__device__ __forceinline__ void st128(u32x4 v, rsrc_t r, unsigned vo) { __builtin_amdgcn_raw_buffer_store_b128(v, r, vo, 0, AUX_SC1); }
__device__ __forceinline__ void st64(u32x2 v, rsrc_t r, unsigned vo) { __builtin_amdgcn_raw_buffer_store_b64(v, r, vo, 0, AUX_SC1); }

__device__ __forceinline__ float asf(unsigned u) { return __uint_as_float(u); }
__device__ __forceinline__ unsigned asu(float f) { return __float_as_uint(f); }
__device__ __forceinline__ f32x4 asf4(u32x4 v) { return __builtin_bit_cast(f32x4, v); }
__device__ __forceinline__ float lo16(unsigned u) { return asf(u << 16); }
__device__ __forceinline__ float hi16(unsigned u) { return asf(u & 0xFFFF0000u); }
__device__ __forceinline__ unsigned pk(float lo, float hi) { bf16x2 v = {(__bf16)lo, (__bf16)hi}; return __builtin_bit_cast(unsigned, v); }
__device__ __forceinline__ float bfr(float f) { return (float)(__bf16)f; }
__device__ __forceinline__ float dot2(unsigned a, unsigned b, float acc) {
  return __builtin_amdgcn_fdot2_f32_bf16(__builtin_bit_cast(bf16x2, a), __builtin_bit_cast(bf16x2, b), acc, false);
}
__device__ __forceinline__ f32x4 smf4(u32x4 a, u32x8 b, f32x4 c, int idx) {
  return __builtin_amdgcn_smfmac_f32_16x16x64_bf16(__builtin_bit_cast(bf16x8, a), __builtin_bit_cast(bf16x16, b), c, idx, 0, 0);
}
__device__ __forceinline__ float smf(u32x4 a, u32x8 b, int idx) { return smf4(a, b, f32x4{0.f, 0.f, 0.f, 0.f}, idx)[0]; }
__device__ __forceinline__ float mfma(u32x4 a, u32x4 b) {
  return __builtin_amdgcn_mfma_f32_16x16x32_bf16(__builtin_bit_cast(bf16x8, a), __builtin_bit_cast(bf16x8, b), f32x4{0.f, 0.f, 0.f, 0.f}, 0, 0, 0)[0];
}
template <int N> __device__ __forceinline__ void zero(f32x4 (&d)[N]) {
#pragma unroll
  for (int i = 0; i < N; ++i) { d[i] = f32x4{0.f, 0.f, 0.f, 0.f}; asm volatile("" : "+v"(d[i])); }
}
__device__ __forceinline__ int smf_idx(int odd) { return odd ? 0xEEEE : 0x4444; }
constexpr int hrow(int i) { return 4 * (i & 15) + 2 * (i >> 5) + ((i >> 4) & 1); }
constexpr int krow(int j) { return 32 * (j >> 3) + 8 * ((j >> 1) & 1) + 2 * ((j >> 2) & 1) + (j & 1); }
__device__ __forceinline__ int klane(int lane) { return 16 * (lane >> 5) + 4 * ((lane >> 4) & 1); }
__device__ __forceinline__ unsigned plo(unsigned a, unsigned b) { return __builtin_amdgcn_perm(b, a, 0x05040100u); }
__device__ __forceinline__ unsigned phi(unsigned a, unsigned b) { return __builtin_amdgcn_perm(b, a, 0x07060302u); }
template <int CTRL> __device__ __forceinline__ float dpp(float x) {
  return __builtin_bit_cast(float, __builtin_amdgcn_mov_dpp(__builtin_bit_cast(int, x), CTRL, 0xf, 0xf, true));
}
template <int N> __device__ __forceinline__ unsigned shl(unsigned x) { return (unsigned)__builtin_amdgcn_mov_dpp((int)x, 0x100 + N, 0xf, 0xf, true); }
constexpr int XOR1 = 0xB1, XOR2 = 0x4E, HMIR = 0x141, ROR8 = 0x128, SHL4 = 0x104;
__device__ __forceinline__ float tr32(float a, float b) { auto s = __builtin_amdgcn_permlane32_swap(asu(a), asu(b), false, false); return asf(s[0]) + asf(s[1]); }
__device__ __forceinline__ float tr16(float a, float b) { auto s = __builtin_amdgcn_permlane16_swap(asu(a), asu(b), false, false); return asf(s[0]) + asf(s[1]); }
__device__ __forceinline__ float tr8(float a, float b, bool b3) { const float t1 = a + dpp<ROR8>(a), t2 = b + dpp<ROR8>(b); return b3 ? t2 : t1; }
__device__ __forceinline__ float sum8(float x) { x += dpp<XOR1>(x); x += dpp<XOR2>(x); x += dpp<HMIR>(x); return x; }
__device__ __forceinline__ float max8(float x) { x = fmaxf(x, dpp<XOR1>(x)); x = fmaxf(x, dpp<XOR2>(x)); x = fmaxf(x, dpp<HMIR>(x)); return x; }
__device__ __forceinline__ float sumg(float x) { x += dpp<ROR8>(x); x = tr16(x, x); return tr32(x, x); }
__device__ __forceinline__ float maxg(float x) {
  x = fmaxf(x, dpp<ROR8>(x));
  auto s = __builtin_amdgcn_permlane16_swap(asu(x), asu(x), false, false); x = fmaxf(asf(s[0]), asf(s[1]));
  auto t = __builtin_amdgcn_permlane32_swap(asu(x), asu(x), false, false); return fmaxf(asf(t[0]), asf(t[1]));
}
__device__ __forceinline__ float sum64(float x) { return sumg(sum8(x)); }
__device__ __forceinline__ float sum21(float x) { x += dpp<SHL4>(x); x += dpp<XOR2>(x); return x; }
__device__ __forceinline__ float rdl(float v, int l) { return asf((unsigned)__builtin_amdgcn_readlane((int)asu(v), l)); }
__device__ __forceinline__ float rstd(float var) { return __builtin_amdgcn_rcpf(bfr(__builtin_amdgcn_sqrtf(bfr(var + 1e-5f)))); }
__device__ __forceinline__ void bar() {
  __builtin_amdgcn_sched_barrier(0);
  asm volatile("s_waitcnt lgkmcnt(0)\n\ts_barrier" ::: "memory");
  __builtin_amdgcn_sched_barrier(0);
}
#define SCHED() __builtin_amdgcn_sched_barrier(0)
#define LEAVE() asm volatile("s_endpgm" ::: "memory")
#define POLL_LOOP _Pragma("nounroll") for (int spin = 0; spin < SPIN_CAP; ++spin)
#define VMWAIT(n) asm volatile("s_waitcnt vmcnt(%0)" ::"n"(n) : "memory")
#define PACE(W, STEP) asm volatile("s_waitcnt vmcnt(%2) lgkmcnt(0)\n\ts_lshl_b32 s94, s92, 4\n\ts_sub_u32 s94, s94, %0\n\ts_add_u32 %0, %0, %1\n\ts_cmp_lt_i32 s94, 0\n\ts_cbranch_scc0 1f\n\t" \
    "s_waitcnt vmcnt(%3)\n\ts_cmp_lt_i32 s94, %5\n\ts_cbranch_scc0 1f\n\ts_waitcnt vmcnt(%4)\n1:\n\ts_memrealtime s[92:93]" \
    : "+s"(due) : "s"(STEP), "n"((W) - 1), "n"(WS - 1), "n"(WS2 - 1), "n"(-16 * D2) : "s92", "s93", "s94", "scc", "memory")
#define HARD(W, STEP) asm volatile("s_waitcnt vmcnt(%2)\n\ts_add_u32 %0, %0, %1\n1:\n\ts_waitcnt lgkmcnt(0)\n\ts_lshl_b32 s94, s92, 4\n\ts_memrealtime s[92:93]\n\ts_sub_u32 s94, s94, %0\n\t" \
    "s_cmp_lt_i32 s94, 0\n\ts_cbranch_scc1 1b" : "+s"(due) : "s"(STEP), "n"((W) - 1) : "s92", "s93", "s94", "scc", "memory")
#define GATE(W, STEP, I) do { if ((I) >= HP) HARD(W, STEP); else PACE(W, STEP); } while (0)
constexpr float L2E = 1.4426950408889634f;

__device__ __forceinline__ u32x4 make_tag(u32x2 e1) {
  const unsigned a0 = e1[0] & 0x3FFu;
  return u32x4{a0, ((e1[0] >> 10) + a0) & 0x3FFu, ((e1[0] >> 20) + a0) & 0x3FFu, (((e1[0] >> 30) | (e1[1] << 2)) + a0) & 0x3FFu};
}
__device__ __forceinline__ u32x4 tagged(f32x4 o, u32x4 T) {
  u32x4 r;
#pragma unroll
  for (int k = 0; k < 4; ++k) r[k] = __builtin_amdgcn_bitop3_b32(asu(o[k]), ~0x3FFu, T[k], 0xEA);
  return r;
}
template <int N, int S, int MODE, int K> __device__ __forceinline__ void poll(rsrc_t r, const unsigned (&off)[N], u32x4 T, u32x4 (&v)[N]);
template <> __device__ __forceinline__ void poll<1, 0, 0, 0>(rsrc_t r, const unsigned (&off)[1], u32x4 T, u32x4 (&v)[1]) {
  asm volatile(
      "s_mov_b64 s[88:89], exec\ns_mov_b32 s90, 0x20000\nv_cmp_ne_u32 vcc, 0x7f000000, %1\ns_mov_b64 s[80:81], vcc\n.Lp_%=:\ns_mov_b64 exec, s[80:81]\n"
      "s_cbranch_execz .Ls0_%=\nbuffer_load_dwordx4 v[160:163], %1, %6, 0 offen sc1\n.Ls0_%=:\ns_mov_b64 exec, s[88:89]\ns_waitcnt vmcnt(0)\nv_xor_b32 v164, v161, %3\n"
      "v_bitop3_b32 v164, v162, %4, v164 bitop3:0xbe\nv_bitop3_b32 v164, v163, %5, v164 bitop3:0xbe\nv_bitop3_b32 v164, v160, %2, v164 bitop3:0xbe\n"
      "v_and_b32 v164, 0x3ff, v164\nv_cmp_ne_u32 vcc, 0, v164\ns_and_b64 s[80:81], s[80:81], vcc\ns_cbranch_scc0 .Le_%=\ns_sub_u32 s90, s90, 1\ns_cbranch_scc0 .Lp_%=\n"
      ".Le_%=:\n"
      : "+&{v[160:163]}"(v[0])
      : "v"(off[0]), "v"(T[0]), "v"(T[1]), "v"(T[2]), "v"(T[3]), "s"(r)
      : "memory", "vcc", "scc", "v164", "s80", "s81", "s88", "s89", "s90");
}
template <> __device__ __forceinline__ void poll<1, 1, 0, 0>(rsrc_t r, const unsigned (&off)[1], u32x4 T, u32x4 (&v)[1]) {
  asm volatile(
      "s_mov_b64 s[88:89], exec\ns_mov_b32 s90, 0x20000\nv_cmp_ne_u32 vcc, 0x7f000000, %1\ns_mov_b64 s[80:81], vcc\n.Lp_%=:\ns_mov_b64 exec, s[80:81]\n"
      "s_cbranch_execz .Ls0_%=\nbuffer_load_dwordx4 v[160:163], %1, %6, 0 offen sc1\n.Ls0_%=:\ns_mov_b64 exec, s[88:89]\ns_waitcnt vmcnt(0)\nv_xor_b32 v164, v161, %3\n"
      "v_bitop3_b32 v164, v162, %4, v164 bitop3:0xbe\nv_bitop3_b32 v164, v163, %5, v164 bitop3:0xbe\nv_bitop3_b32 v164, v160, %2, v164 bitop3:0xbe\n"
      "v_and_b32 v164, 0x3ff, v164\nv_cmp_ne_u32 vcc, 0, v164\ns_and_b64 s[80:81], s[80:81], vcc\ns_cbranch_scc0 .Le_%=\ns_sleep 1\ns_sub_u32 s90, s90, 1\n"
      "s_cbranch_scc0 .Lp_%=\n.Le_%=:\n"
      : "+&{v[160:163]}"(v[0])
      : "v"(off[0]), "v"(T[0]), "v"(T[1]), "v"(T[2]), "v"(T[3]), "s"(r)
      : "memory", "vcc", "scc", "v164", "s80", "s81", "s88", "s89", "s90");
}
template <> __device__ __forceinline__ void poll<1, 4, 0, 0>(rsrc_t r, const unsigned (&off)[1], u32x4 T, u32x4 (&v)[1]) {
  asm volatile(
      "s_mov_b64 s[88:89], exec\ns_mov_b32 s90, 0x20000\nv_cmp_ne_u32 vcc, 0x7f000000, %1\ns_mov_b64 s[80:81], vcc\n.Lp_%=:\ns_mov_b64 exec, s[80:81]\n"
      "s_cbranch_execz .Ls0_%=\nbuffer_load_dwordx4 v[160:163], %1, %6, 0 offen sc1\n.Ls0_%=:\ns_mov_b64 exec, s[88:89]\ns_waitcnt vmcnt(0)\nv_xor_b32 v164, v161, %3\n"
      "v_bitop3_b32 v164, v162, %4, v164 bitop3:0xbe\nv_bitop3_b32 v164, v163, %5, v164 bitop3:0xbe\nv_bitop3_b32 v164, v160, %2, v164 bitop3:0xbe\n"
      "v_and_b32 v164, 0x3ff, v164\nv_cmp_ne_u32 vcc, 0, v164\ns_and_b64 s[80:81], s[80:81], vcc\ns_cbranch_scc0 .Le_%=\ns_sleep 4\ns_sub_u32 s90, s90, 1\n"
      "s_cbranch_scc0 .Lp_%=\n.Le_%=:\n"
      : "+&{v[160:163]}"(v[0])
      : "v"(off[0]), "v"(T[0]), "v"(T[1]), "v"(T[2]), "v"(T[3]), "s"(r)
      : "memory", "vcc", "scc", "v164", "s80", "s81", "s88", "s89", "s90");
}
template <> __device__ __forceinline__ void poll<2, 2099201, 5, 0>(rsrc_t r, const unsigned (&off)[2], u32x4 T, u32x4 (&v)[2]) {
  asm volatile(
      "s_mov_b64 s[88:89], exec\ns_mov_b32 s90, 0x20000\nv_cmp_ne_u32 vcc, 0x7f000000, %2\ns_mov_b64 s[80:81], vcc\nv_cmp_ne_u32 vcc, 0x7f000000, %3\n"
      "s_mov_b64 s[82:83], vcc\ns_branch .Lc_%=\n.Lp_%=:\ns_mov_b64 exec, s[80:81]\ns_cbranch_execz .Ls0_%=\nbuffer_load_dwordx4 v[156:159], %2, %8, 0 offen sc1\n"
      ".Ls0_%=:\ns_mov_b64 exec, s[82:83]\ns_cbranch_execz .Ls1_%=\nbuffer_load_dwordx4 v[160:163], %3, %8, 0 offen sc1\n.Ls1_%=:\ns_mov_b64 exec, s[88:89]\n"
      "s_waitcnt vmcnt(0)\n.Lc_%=:\nv_xor_b32 v164, v157, %5\nv_bitop3_b32 v164, v158, %6, v164 bitop3:0xbe\nv_bitop3_b32 v164, v159, %7, v164 bitop3:0xbe\n"
      "v_bitop3_b32 v164, v156, %4, v164 bitop3:0xbe\nv_and_b32 v164, 0x3ff, v164\nv_cmp_ne_u32 vcc, 0, v164\ns_and_b64 s[80:81], s[80:81], vcc\n"
      "v_xor_b32 v164, v161, %5\nv_bitop3_b32 v164, v162, %6, v164 bitop3:0xbe\nv_bitop3_b32 v164, v163, %7, v164 bitop3:0xbe\n"
      "v_bitop3_b32 v164, v160, %4, v164 bitop3:0xbe\nv_and_b32 v164, 0x3ff, v164\nv_cmp_ne_u32 vcc, 0, v164\ns_and_b64 s[82:83], s[82:83], vcc\n"
      "s_or_b64 s[92:93], s[80:81], s[82:83]\ns_cbranch_scc0 .Le_%=\ns_bcnt1_i32_b64 s96, s[80:81]\ns_mov_b32 s91, s96\ns_bcnt1_i32_b64 s96, s[82:83]\n"
      "s_add_u32 s91, s91, s96\ns_cmp_le_u32 s91, 32\ns_cbranch_scc1 .Lq_%=\ns_sleep 8\n.Lq_%=:\ns_sleep 1\ns_sub_u32 s90, s90, 1\ns_cbranch_scc0 .Lp_%=\n.Le_%=:\n"
      : "+&{v[156:159]}"(v[0]), "+&{v[160:163]}"(v[1])
      : "v"(off[0]), "v"(off[1]), "v"(T[0]), "v"(T[1]), "v"(T[2]), "v"(T[3]), "s"(r)
      : "memory", "vcc", "scc", "v164", "s80", "s81", "s82", "s83", "s88", "s89", "s90", "s92", "s93", "s91", "s96");
}
template <> __device__ __forceinline__ void poll<3, 10, 0, 0>(rsrc_t r, const unsigned (&off)[3], u32x4 T, u32x4 (&v)[3]) {
  asm volatile(
      "s_mov_b64 s[88:89], exec\ns_mov_b32 s90, 0x20000\nv_cmp_ne_u32 vcc, 0x7f000000, %3\ns_mov_b64 s[80:81], vcc\nv_cmp_ne_u32 vcc, 0x7f000000, %4\n"
      "s_mov_b64 s[82:83], vcc\nv_cmp_ne_u32 vcc, 0x7f000000, %5\ns_mov_b64 s[84:85], vcc\n.Lp_%=:\ns_mov_b64 exec, s[80:81]\ns_cbranch_execz .Ls0_%=\n"
      "buffer_load_dwordx4 v[152:155], %3, %10, 0 offen sc1\n.Ls0_%=:\ns_mov_b64 exec, s[82:83]\ns_cbranch_execz .Ls1_%=\n"
      "buffer_load_dwordx4 v[156:159], %4, %10, 0 offen sc1\n.Ls1_%=:\ns_mov_b64 exec, s[84:85]\ns_cbranch_execz .Ls2_%=\n"
      "buffer_load_dwordx4 v[160:163], %5, %10, 0 offen sc1\n.Ls2_%=:\ns_mov_b64 exec, s[88:89]\ns_waitcnt vmcnt(0)\nv_xor_b32 v164, v153, %7\n"
      "v_bitop3_b32 v164, v154, %8, v164 bitop3:0xbe\nv_bitop3_b32 v164, v155, %9, v164 bitop3:0xbe\nv_bitop3_b32 v164, v152, %6, v164 bitop3:0xbe\n"
      "v_and_b32 v164, 0x3ff, v164\nv_cmp_ne_u32 vcc, 0, v164\ns_and_b64 s[80:81], s[80:81], vcc\nv_xor_b32 v164, v157, %7\n"
      "v_bitop3_b32 v164, v158, %8, v164 bitop3:0xbe\nv_bitop3_b32 v164, v159, %9, v164 bitop3:0xbe\nv_bitop3_b32 v164, v156, %6, v164 bitop3:0xbe\n"
      "v_and_b32 v164, 0x3ff, v164\nv_cmp_ne_u32 vcc, 0, v164\ns_and_b64 s[82:83], s[82:83], vcc\ns_or_b64 s[92:93], s[80:81], s[82:83]\nv_xor_b32 v164, v161, %7\n"
      "v_bitop3_b32 v164, v162, %8, v164 bitop3:0xbe\nv_bitop3_b32 v164, v163, %9, v164 bitop3:0xbe\nv_bitop3_b32 v164, v160, %6, v164 bitop3:0xbe\n"
      "v_and_b32 v164, 0x3ff, v164\nv_cmp_ne_u32 vcc, 0, v164\ns_and_b64 s[84:85], s[84:85], vcc\ns_or_b64 s[92:93], s[92:93], s[84:85]\ns_cbranch_scc0 .Le_%=\n"
      "s_sleep 10\ns_sub_u32 s90, s90, 1\ns_cbranch_scc0 .Lp_%=\n.Le_%=:\n"
      : "+&{v[152:155]}"(v[0]), "+&{v[156:159]}"(v[1]), "+&{v[160:163]}"(v[2])
      : "v"(off[0]), "v"(off[1]), "v"(off[2]), "v"(T[0]), "v"(T[1]), "v"(T[2]), "v"(T[3]), "s"(r)
      : "memory", "vcc", "scc", "v164", "s80", "s81", "s82", "s83", "s84", "s85", "s88", "s89", "s90", "s92", "s93");
}
template <> __device__ __forceinline__ void poll<3, 4195840, 0, 0>(rsrc_t r, const unsigned (&off)[3], u32x4 T, u32x4 (&v)[3]) {
  asm volatile(
      "s_mov_b64 s[88:89], exec\ns_mov_b32 s90, 0x20000\nv_cmp_ne_u32 vcc, 0x7f000000, %3\ns_mov_b64 s[80:81], vcc\nv_cmp_ne_u32 vcc, 0x7f000000, %4\n"
      "s_mov_b64 s[82:83], vcc\nv_cmp_ne_u32 vcc, 0x7f000000, %5\ns_mov_b64 s[84:85], vcc\n.Lp_%=:\ns_mov_b64 exec, s[80:81]\ns_cbranch_execz .Ls0_%=\n"
      "buffer_load_dwordx4 v[152:155], %3, %10, 0 offen sc1\n.Ls0_%=:\ns_mov_b64 exec, s[82:83]\ns_cbranch_execz .Ls1_%=\n"
      "buffer_load_dwordx4 v[156:159], %4, %10, 0 offen sc1\n.Ls1_%=:\ns_mov_b64 exec, s[84:85]\ns_cbranch_execz .Ls2_%=\n"
      "buffer_load_dwordx4 v[160:163], %5, %10, 0 offen sc1\n.Ls2_%=:\ns_mov_b64 exec, s[88:89]\ns_waitcnt vmcnt(0)\nv_xor_b32 v164, v153, %7\n"
      "v_bitop3_b32 v164, v154, %8, v164 bitop3:0xbe\nv_bitop3_b32 v164, v155, %9, v164 bitop3:0xbe\nv_bitop3_b32 v164, v152, %6, v164 bitop3:0xbe\n"
      "v_and_b32 v164, 0x3ff, v164\nv_cmp_ne_u32 vcc, 0, v164\ns_and_b64 s[80:81], s[80:81], vcc\nv_xor_b32 v164, v157, %7\n"
      "v_bitop3_b32 v164, v158, %8, v164 bitop3:0xbe\nv_bitop3_b32 v164, v159, %9, v164 bitop3:0xbe\nv_bitop3_b32 v164, v156, %6, v164 bitop3:0xbe\n"
      "v_and_b32 v164, 0x3ff, v164\nv_cmp_ne_u32 vcc, 0, v164\ns_and_b64 s[82:83], s[82:83], vcc\ns_or_b64 s[92:93], s[80:81], s[82:83]\nv_xor_b32 v164, v161, %7\n"
      "v_bitop3_b32 v164, v162, %8, v164 bitop3:0xbe\nv_bitop3_b32 v164, v163, %9, v164 bitop3:0xbe\nv_bitop3_b32 v164, v160, %6, v164 bitop3:0xbe\n"
      "v_and_b32 v164, 0x3ff, v164\nv_cmp_ne_u32 vcc, 0, v164\ns_and_b64 s[84:85], s[84:85], vcc\ns_or_b64 s[92:93], s[92:93], s[84:85]\ns_cbranch_scc0 .Le_%=\n"
      "s_bcnt1_i32_b64 s96, s[80:81]\ns_mov_b32 s91, s96\ns_bcnt1_i32_b64 s96, s[82:83]\ns_add_u32 s91, s91, s96\ns_bcnt1_i32_b64 s96, s[84:85]\ns_add_u32 s91, s91, s96\n"
      "s_cmp_le_u32 s91, 64\ns_cbranch_scc1 .Lq_%=\ns_sleep 6\n.Lq_%=:\ns_sub_u32 s90, s90, 1\ns_cbranch_scc0 .Lp_%=\n.Le_%=:\n"
      : "+&{v[152:155]}"(v[0]), "+&{v[156:159]}"(v[1]), "+&{v[160:163]}"(v[2])
      : "v"(off[0]), "v"(off[1]), "v"(off[2]), "v"(T[0]), "v"(T[1]), "v"(T[2]), "v"(T[3]), "s"(r)
      : "memory", "vcc", "scc", "v164", "s80", "s81", "s82", "s83", "s84", "s85", "s88", "s89", "s90", "s92", "s93", "s91", "s96");
}
template <int STRIDE, int S, int MODE = 0, int K = 0, int N> __device__ __forceinline__ void gather(rsrc_t rws, unsigned off0, u32x4 T, f32x4 (&o)[N]) {
  unsigned off[N];
  u32x4 v[N];
#pragma unroll
  for (int j = 0; j < N; ++j) { off[j] = off0 >= OOB ? OOB : off0 + j * STRIDE; v[j] = T; }
  SCHED();
  poll<N, S, MODE, K>(rws, off, T, v);
  SCHED();
#pragma unroll
  for (int j = 0; j < N; ++j) o[j] = asf4(v[j] ^ T);
}
__device__ __forceinline__ void turn(const u32x4* w, u32x8* o) {
#pragma unroll
  for (int k = 0; k < 4; ++k)
#pragma unroll
    for (int r = 0; r < 8; ++r) { o[2 * k][r] = plo(w[2 * r][k], w[2 * r + 1][k]); o[2 * k + 1][r] = phi(w[2 * r][k], w[2 * r + 1][k]); }
#pragma unroll
  for (int i = 0; i < 8; ++i) asm volatile("" : "+v"(o[i]));
}
constexpr int krow2(int t) { return 32 * (t >> 2) + (t & 3); }
__device__ __forceinline__ void turn2(const u32x4* w, u32x8* o) {
#pragma unroll
  for (int k = 0; k < 4; ++k)
#pragma unroll
    for (int r = 0; r < 4; ++r) { o[k][2 * r] = plo(w[2 * r][k], w[2 * r + 1][k]); o[k][2 * r + 1] = phi(w[2 * r][k], w[2 * r + 1][k]); }
#pragma unroll
  for (int i = 0; i < 4; ++i) asm volatile("" : "+v"(o[i]));
}
template <int S = 2> __device__ __forceinline__ void lds_wait(unsigned* w, unsigned want) {
  POLL_LOOP {
    if (__hip_atomic_load(w, __ATOMIC_RELAXED, __HIP_MEMORY_SCOPE_WORKGROUP) >= want) break;
    if (S) __builtin_amdgcn_s_sleep(S);
  }
  asm volatile("" ::: "memory");
}
__device__ __forceinline__ void lds_post(unsigned* w, int lane, unsigned n = 1u) {
  asm volatile("s_waitcnt lgkmcnt(0)" ::: "memory");
  if (lane == 0) __hip_atomic_fetch_add(w, n, __ATOMIC_RELAXED, __HIP_MEMORY_SCOPE_WORKGROUP);
}

__device__ __forceinline__ void raise(unsigned* w) { asm volatile("" ::: "memory"); __hip_atomic_store(w, 1u, __ATOMIC_RELAXED, __HIP_MEMORY_SCOPE_WORKGROUP); }
template <int S> __device__ __forceinline__ void await(unsigned* w, int n) {
  POLL_LOOP {
    if (__builtin_popcountll(__builtin_amdgcn_ballot_w64(__hip_atomic_load(w, __ATOMIC_RELAXED, __HIP_MEMORY_SCOPE_WORKGROUP) != 0u)) >= n) break;
    if (S) __builtin_amdgcn_s_sleep(S);
  }
  asm volatile("" ::: "memory");
}
__device__ __forceinline__ float score(const u32x4* k, const u32x4* q) {
  float d0 = 0.f, d1 = 0.f;
#pragma unroll
  for (int j = 0; j < 3; ++j) {
    d0 = dot2(k[j][0], q[j][0], d0); d1 = dot2(k[j][1], q[j][1], d1);
    d0 = dot2(k[j][2], q[j][2], d0); d1 = dot2(k[j][3], q[j][3], d1);
  }
  return bfr(bfr(sum8(d0 + d1)) * 0.07216878364870322f);
}
__device__ __forceinline__ void pv8(u32x4 v, unsigned pl, float* acc) {
  const unsigned ph = pl << 16;
#pragma unroll
  for (int k = 0; k < 4; ++k) { acc[2 * k] = dot2(v[k], pl, acc[2 * k]); acc[2 * k + 1] = dot2(v[k], ph, acc[2 * k + 1]); }
}
__device__ __forceinline__ float fold8(const float* acc, int lane) {
  return tr8(tr16(tr32(acc[0], acc[4]), tr32(acc[2], acc[6])), tr16(tr32(acc[1], acc[5]), tr32(acc[3], acc[7])), lane & 8);
}

struct __attribute__((aligned(16))) Lds {
  unsigned dump[64];
  float ored[NSW][DH];
  float pred[NSW + 1][64];
  float fred[10][128];
  float cred[10][128];
  unsigned short qs[DH];
  unsigned short act[72];
  float ml[NSW][2];
  unsigned tag[4];
  unsigned cnt[4];
  unsigned flag[24];
  unsigned hsum[2][32];
  unsigned near;
  unsigned pad[1];
  u32x4 wst[2][512][8];
};

struct Plan { unsigned w[3 * NWG + 2]; };
int job_of(int h, int s) {
  const int e = (s - h - 1) & 7;
  return h >= NF ? h * 8 + s : e == 7 ? h : e >= 2 ? NF + s * 15 + (h >> 3) * 5 + e - 2 : NF + NP + 2 * h + e;
}
#define UNDEF8(w) asm volatile("" : "=v"(w[0]), "=v"(w[1]), "=v"(w[2]), "=v"(w[3]), "=v"(w[4]), "=v"(w[5]), "=v"(w[6]), "=v"(w[7]))
#define LOFF(x) (unsigned)((const char*)&(x) - (const char*)&L)
#define LAT(T, off) ((T*)((char*)&L + (off)))
typedef const unsigned short* cptr;
typedef const __attribute__((address_space(1))) unsigned short* gptr;
typedef const __attribute__((address_space(4))) unsigned long long* kargs_t;
#define KARGS kargs_t ka = (kargs_t)__builtin_amdgcn_kernarg_segment_ptr(); asm volatile("" : "+s"(ka))
#define ARG(i) ((gptr)ka[i])
__global__ __launch_bounds__(NTHR, 3) void k_fused(cptr, cptr, cptr, cptr, cptr, cptr, cptr, cptr, unsigned short*, unsigned char*, Plan) {
  __shared__ Lds L;
  asm volatile(".Lcode_begin:\n\ts_mul_i32 vcc_lo, %1, 12\n\ts_load_dwordx4 s[96:99], %0, 0x8\n\ts_load_dwordx4 s[92:95], %0, vcc_lo offset:0x50\n\ts_load_dwordx2 s[100:101], %0, 0x18"
               ::"s"(__builtin_amdgcn_kernarg_segment_ptr()), "s"(blockIdx.x) : "vcc", "s92", "s93", "s94", "s95", "s96", "s97", "s98", "s99", "s100", "s101");
  SCHED();
  const unsigned tid0 = __builtin_amdgcn_readfirstlane(threadIdx.x);
  int wave = tid0 >> 6;
  {
    unsigned t;
    asm volatile("s_or_b32 s88, %1, %2\n\ts_cmp_lt_u32 s88, %3\n\ts_cbranch_scc0 1f\n\ts_getpc_b64 s[88:89]\n\ts_mov_b32 m0, %4\n\tv_lshlrev_b32 %0, 7, %5\n\ts_andn2_b32 s88, s88, 0xff\n\ts_and_b32 s89, s89, 0xffff\n\t"
                 "s_mov_b32 s90, .Lcode_end-.Lcode_begin\n\ts_max_u32 s90, s90, .Lcode_end2-.Lcode_begin\n\ts_max_u32 s90, s90, .Lcode_end3-.Lcode_begin\n\ts_max_u32 s90, s90, .Lcode_end4-.Lcode_begin\n\ts_mov_b32 s91, 0x20000\n\t"
                 "s_movk_i32 s87, 0x2000\n\tbuffer_load_dword %0, s[88:91], 0 offen lds\n\tbuffer_load_dword %0, s[88:91], s87 offen lds\n\ts_movk_i32 s87, 0x4000\n\t"
                 "buffer_load_dword %0, s[88:91], s87 offen lds\n1:"
                 : "=&v"(t) : "s"(blockIdx.x), "s"(tid0), "n"(CPF), "n"(0), "v"(threadIdx.x) : "memory", "scc", "s87", "s88", "s89", "s90", "s91");
    SCHED();
  }
  u32x4 first[6];
  gptr kc0, vc0, wq;
  unsigned kvo, plan, vq;
  unsigned long long tk0;
  {
    const bool st = wave < NSW;
    unsigned so[6], idx = (threadIdx.x >> 3) & 63u, c16 = (threadIdx.x & 7u) * 16u, size = st ? 32u * SK * DH * 2u : D * 6144u * 2u;
#pragma unroll
    for (int i = 1; i < 6; ++i) so[i] = st ? (i % 3) * 128 + (i / 3) * 24576 : i * 12288;
    asm volatile("" : "+s"(so[1]), "+s"(so[2]), "+s"(so[3]), "+s"(so[4]), "+s"(so[5]), "+s"(size));
    asm volatile("" : "+v"(idx), "+v"(c16));
    gptr p;
    asm volatile("s_cmp_lt_u32 %0, 8\n\ts_cbranch_scc1 1f\n\ts_setprio 3\n1:" ::"s"(wave) : "scc");
    asm volatile("s_cmp_lt_u32 %3, 8\n\ts_waitcnt lgkmcnt(0)\n\ts_cselect_b32 s93, s92, s93\n\tv_add_u32 %1, s93, %4\n\ts_cselect_b64 %0, s[96:97], s[100:101]\n\t"
                 "s_cselect_b32 s95, 0, s94\n\ts_bfe_u32 s95, s95, 0x4000a\n\ts_mul_i32 s95, s95, 0x3000\n\ts_max_u32 s95, s95, 0x180\n\tv_mad_u32_u24 %1, %2, s95, %1"
                 : "=&s"(p), "=&v"(vq) : "v"(idx), "s"(wave), "v"(c16) : "scc", "s93", "s95");
    const rsrc_t r = mk(p, size);
#pragma unroll
    for (int i = 0; i < 6; ++i) {
      if (i == 0) { SCHED(); tk0 = __builtin_amdgcn_s_memrealtime(); SCHED(); }
      first[i] = ld128<AUX_NT>(r, vq, i ? so[i] : 0u);
    }
    SCHED();
    asm volatile("s_mov_b64 %0, s[96:97]\n\ts_mov_b64 %1, s[98:99]\n\ts_mov_b32 %2, s92\n\ts_mov_b32 %3, s94" : "=s"(kc0), "=s"(vc0), "=s"(kvo), "=s"(plan));
    wq = p;
  }
  int lane = threadIdx.x & 63, b = blockIdx.x;
  asm volatile("" : "+v"(lane));
  asm volatile("" : "+s"(wave), "+s"(b), "+s"(plan));
  const int h = b >> 3, s = b & 7;
  const int g = lane >> 3, c = lane & 7, tk = plan >> 16;
  const bool stream = wave < NSW, isF = tk < NF;
  const int a = wave - NSW;
  u32x2 e1 = {0u, 0u};
  u32x4 T = {0u, 0u, 0u, 0u};
  u32x4 wt[8];

  if (__builtin_expect(stream, 0)) {
    if (wave < 2) {
      const unsigned t = wave * 64 + lane, i = t - 12u;
      if (t < 94u) L.cnt[t] = i < 16u ? i >= 10u : i < 80u && ((i - 16u) & 31u) < 20u ? ~0u : 0u;
    }
    const int nk = plan & 1023;
    const unsigned voff = vq - kvo;
    const rsrc_t rk = mk(kc0 + kvo / 2, nk * 384), rv = mk(vc0 + kvo / 2, nk * 384);
    u32x4 kb[27], vb[27];
    unsigned due, tau, tau8;
#pragma unroll
    for (int i = 0; i < 27; ++i) {
      if (i == KW) bar();
      if (i == KW) {
        const bool nine = 512 + 8 * wave < nk;
        tau = isF ? 0u : tk < NF + NP ? (nine ? 16 * (SP - SA) / 53 : 16 * (SP - SA) / 47) : nine ? 16 * (SE - SA) / 53 : 16 * (SE - SA) / 47;
        tau8 = nine ? tau : 0u;
        due = ((unsigned)tk0 << 4) + (isF ? -65536 : 16 * SA) + (KW - 1) * tau;
        asm volatile("s_memrealtime s[92:93]" : "+s"(tau), "+s"(tau8) :: "s92", "s93");
      }
      if (i >= KW) GATE(KW, i < 24 ? tau : tau8, i);
      kb[i] = i < 6 ? first[i] : ld128<AUX_NT>(rk, voff + (i % 3) * 128, (i / 3) * 24576);
      SCHED();
    }
#define LISTLOAD(n) vb[n] = ld128<AUX_NT>(rv, voff + ((n) % 3) * 128, (8 - (n) / 3) * 24576)
#pragma unroll
    for (int n = 0; n < VW; ++n) {
      GATE(KW, n < 3 ? tau8 : tau, 27 + n);
      LISTLOAD(n);
      SCHED();
    }
    lds_wait(&L.cnt[1], 1);
    u32x4 qv[3];
#pragma unroll
    for (int j = 0; j < 3; ++j) qv[j] = *(const u32x4*)&L.qs[j * 64 + c * 8];
    float sc[9];
#pragma unroll
    for (int t = 0; t < 9; ++t) {
      sc[t] = score(kb + 3 * t, qv);
      if (t & 1) {
        SCHED();
#pragma unroll
        for (int n = VW + VN * (t - 1) / 8; n < VW + VN * (t + 1) / 8; ++n) LISTLOAD(n);
        SCHED();
      }
    }
    due += VN * tau;
    asm volatile("s_memrealtime s[92:93]" : "+s"(due) :: "s92", "s93");
    constexpr int V1 = VW + VN;
#pragma unroll
    for (int t = 1; t < 9; ++t) if (64 * t + 8 * wave >= nk) sc[t] = -__builtin_inff();
    float m = sc[0];
#pragma unroll
    for (int t = 1; t < 9; ++t) m = fmaxf(m, sc[t]);
    m = maxg(m);
    float lsum = 0.f;
    unsigned pp[9];
#pragma unroll
    for (int t = 0; t < 9; ++t) { const float p = __builtin_amdgcn_exp2f((sc[t] - m) * L2E); lsum += p; pp[t] = pk(p, 0.f); }
    lsum = sumg(lsum);
    if (lane == 0) *(f32x2*)L.ml[wave] = f32x2{m, lsum};
    lds_post(&L.cnt[0], lane);
    float acc[24];
    unsigned odst = LOFF(L.ored[wave][8 * c + g]);
    KARGS;
    const rsrc_t rw = mk(isF ? ARG(7) : ARG(4), isF ? FF * D * 2 : tk < NF + NP ? 6144 * D * 2 : 0);
    unsigned wvo = (unsigned)(isF ? klane(lane) + (lane & 8) : (lane >> 4) + (lane & 8)) * 1280u + c * 16u;
    int ws0 = isF ? 64 * tk * 1280 + wave * 128 : ((tk - NF) / 10 * 512 + 64 * wave) * 1280 + (tk - NF) % 10 * 128, ws1 = isF ? 1280 : 4 * 1280, ws2 = isF ? 2 * 1280 : 16 * 1280;
    const rsrc_t rws = mk(ARG(9), WS_END);
    unsigned oo = WS_O - 1u + (unsigned)g * O_ROW + orow((unsigned)((tk - NF) / 10 * 512 + 64 * wave + 4 * c) * 4u);
    asm volatile("" : "+v"(odst), "+v"(wvo), "+v"(oo));
    asm volatile("" : "+s"(ws0), "+s"(ws1), "+s"(ws2));
#define WLOAD(t) wt[t] = ld128<AUX_NT>(rw, wvo, ws0 + ((t) & 1) * ws1 + (((t) >> 1) & 1) * ws2 + ((t) >> 2) * 32 * 1280)
#pragma unroll
    for (int e = 0; e < 24; ++e) acc[e] = 0.f;
#pragma unroll
    for (int n = 0; n < 27; ++n) {
      if (n + VP >= V1 && n + VP < 27) { GATE(VP, tau, 27 + n + VP); LISTLOAD(n + VP); }
      SCHED();
      pv8(vb[n], pp[8 - n / 3], acc + 8 * (n % 3));
      if (n + WW >= 27 && n < 26) { SCHED(); WLOAD(n + WW - 27); SCHED(); }
      if (n >= 24) *LAT(float, odst + (n % 3) * 256) = fold8(acc + 8 * (n % 3), lane);
      SCHED();
    }
    if (tk >= NF + NP) {
      bar();
      LEAVE();
    } else {
      raise(&L.flag[wave]);
    }
    if (isF) {
#pragma unroll
      for (int t = WW - 1; t < 8; ++t) { VMWAIT(W2 - 1); WLOAD(t); SCHED(); }
    } else if (tk < NF + NP) {
      T = *(const u32x4*)L.tag;
      lds_wait(&L.near, 1);
      oo += L.near;
      unsigned off[2] = {oo, oo + 128u}, pdst = LOFF(L.pred[g == 0 ? wave : NSW][8 * c]);
      int idx = smf_idx(lane & 1);
      asm volatile("" : "+v"(pdst), "+v"(idx), "+v"(off[0]), "+v"(off[1]), "+v"(T));
      f32x4 d[4];
      zero(d);
      SCHED();
#pragma unroll
      for (int t = WW - 1; t < 8; ++t) {
        VMWAIT(W2 - 1);
        while ((int)((unsigned)__builtin_amdgcn_s_memrealtime() - ((unsigned)tk0 + SW - (7 - t) * TW)) < 0) __builtin_amdgcn_s_sleep(1);
        WLOAD(t);
        SCHED();
      }
      lds_post(&L.cnt[2], lane);
      lds_wait<1>(&L.cnt[2], NSW);
      VMWAIT(0);
      u32x4 v[2] = {ld128<AUX_SC1>(rws, off[0], 0), ld128<AUX_SC1>(rws, off[1], 0)};
      SCHED();
      u32x8 wp[4];
      turn2(wt, wp);
      SCHED();
      poll<2, OS + 256 * OL + 65536 * OK, 5, 0>(rws, off, T, v);
      SCHED();
      float o8[8];
#pragma unroll
      for (int i = 0; i < 8; ++i) o8[i] = asf(v[i >> 2][i & 3] ^ T[i & 3]);
      const float o1 = fold8(o8, lane);
      const unsigned op = pk(dpp<0xA0>(o1), dpp<0xF5>(o1));
      const u32x4 av = {op, shl<4>(op), shl<8>(op), shl<12>(op)};
#pragma unroll
      for (int k = 0; k < 4; ++k) d[k] = smf4(av, wp[k], d[k], idx);
      SCHED();
#pragma unroll
      for (int k = 0; k < 4; k += 2) {
        f32x4 r = {d[k][0] + dpp<ROR8>(d[k][2]), 0.f, d[k + 1][0] + dpp<ROR8>(d[k + 1][2]), 0.f};
        asm volatile("" : "+v"(r[0]), "+v"(r[2]));
        r[1] = d[k][1] + dpp<ROR8>(d[k][3]); r[3] = d[k + 1][1] + dpp<ROR8>(d[k + 1][3]);
        *LAT(f32x4, pdst + 8 * k) = r;
      }
      bar();
      LEAVE();
      asm volatile(".Lcode_end4:");
    }
  } else if (__builtin_expect(isF && a < 2, 0)) {
    KARGS;
    const rsrc_t rw = mk((a ? ARG(6) : ARG(5)) + 64 * h, D * FF * 2 - 128 * h);
    const unsigned vo = (unsigned)g * 3072u + c * 16u;
    unsigned so = 0u, dst = LOFF(L.wst[a]) + (unsigned)(size_t)&L;
#define DMA() asm volatile("s_mov_b32 m0, %1\n\ts_add_u32 %1, %1, 0x400\n\tbuffer_load_dwordx4 %2, %3, %0 offen nt lds\n\ts_add_u32 %0, %0, 0x6000" : "+s"(so), "+s"(dst) : "v"(vo), "s"(rw) : "memory", "scc")
    bar();
#pragma nounroll
    for (int i = 0; i < 64; ++i) { VMWAIT(DW - 1); DMA(); }
    asm volatile(".Lcode_end3:");
    UNDEF8(wt);
  } else {
    const int nl = (plan >> 10) & 15, qpart = (plan >> 14) & 3;
    const bool big = nl > 8, qjob = nl != 0;
    constexpr int NS = 8;
    const rsrc_t rq = mk(wq, D * 6144 * 2);
    u32x4 wb[NB];
#pragma unroll
    for (int i = 0; i < NS; ++i) wb[i] = i < 6 ? first[i] : ld128<AUX_NT>(rq, nl > 6 ? vq : OOB, i * 12288);
    if (big) {
#pragma unroll
      for (int i = NS; i < NB; ++i) wb[i] = ld128<AUX_NT>(rq, vq, i * 12288);
    }
    const unsigned row = vq / 12288u, qline = __builtin_amdgcn_readfirstlane((vq - row * 12288u) >> 7), xo = qjob ? row * 2u : OOB;
    SCHED();
    KARGS;
    const rsrc_t rws = mk(ARG(9), WS_END), rx = mk(ARG(0), D * 2);
    const u32x4 xs4 = ld128<0>(rx, (unsigned)lane * 16u, 0);
    const unsigned xs1 = ld32<0>(rx, 1024u + (unsigned)lane * 4u, 0);
    const u32x4 xr0 = ld128<0>(rx, xo, 0), xr4 = ld128<0>(rx, xo + 16u, 0);
    const u32x2 ep = ld64<AUX_SC1>(rws, WS_EPOCH, 0);
    unsigned pf[3] = {0u, 0u, 0u};
    if (a == 3) {
      pf[0] = ld32<0>(mk(ARG(8), D * 2), 0, 0);
      const unsigned kvn = (plan & 1023u) * 384u, po = lane == 0 ? 0u : lane == 1 ? kvn - 128u : OOB;
      pf[1] = ld32<AUX_NT>(mk(vc0 + kvo / 2, kvn), po, 0);
      pf[2] = ld32<AUX_NT>(mk(kc0 + kvo / 2, kvn), lane == 1 ? po : OOB, 0);
    }
    bar();
    f32x4 qp = {0.f, 0.f, 0.f, 0.f};
    if (qjob) {
      const unsigned xs[5] = {xs4[0], xs4[1], xs4[2], xs4[3], xs1}, xr[7] = {xr0[0], xr0[1], xr0[2], xr0[3], xr4[0], xr4[1], xr4[2]};
      float sm = 0.f;
#pragma unroll
      for (int i = 0; i < 5; ++i) sm += lo16(xs[i]) + hi16(xs[i]);
      const float mean = sum64(sm) * (1.f / D);
      float sq = 0.f;
#pragma unroll
      for (int i = 0; i < 5; ++i) { const float d0 = lo16(xs[i]) - mean, d1 = hi16(xs[i]) - mean; sq += d0 * d0 + d1 * d1; }
      const float mb = bfr(mean), rden = rstd(bfr(sum64(sq) * (1.f / D)));
      float qa[8];
#pragma unroll
      for (int k = 0; k < 8; ++k) qa[k] = 0.f;
      auto rows = [&](int i) {
        const unsigned xp = pk(bfr(lo16(xr[i]) - mb) * rden, bfr(hi16(xr[i]) - mb) * rden);
#pragma unroll
        for (int k = 0; k < 4; ++k) {
          qa[2 * k] = dot2(plo(wb[2 * i][k], wb[2 * i + 1][k]), xp, qa[2 * k]);
          qa[2 * k + 1] = dot2(phi(wb[2 * i][k], wb[2 * i + 1][k]), xp, qa[2 * k + 1]);
        }
      };
#pragma unroll
      for (int i = 0; i < NS / 2; ++i) rows(i);
      if (big) {
#pragma unroll
        for (int i = NS / 2; i < NB / 2; ++i) rows(i);
      }
#pragma unroll
      for (int i = 0; i < 4; ++i) { float t = tr32(qa[i], qa[i + 4]); t = tr16(t, t); qp[i] = t + dpp<ROR8>(t); }
    }
    if ((lane & 24) == 0) *(f32x4*)&L.pred[a][c * 8 + (lane >> 5) * 4] = qp;
    lds_post(&L.cnt[3], lane);
    const unsigned long long en = (((unsigned long long)ep[1] << 32) | ep[0]) + 1ull;
    e1 = u32x2{(unsigned)en, (unsigned)(en >> 32)};
    T = make_tag(e1);
    if (a == 0 && qjob) {
      lds_wait(&L.cnt[3], 4);
      const float* src = &L.pred[0][4 * (lane & 15)];
      qp = (*(const f32x4*)src + *(const f32x4*)(src + 64)) + (*(const f32x4*)(src + 128) + *(const f32x4*)(src + 192));
      const unsigned ex = asu(fmaxf(fmaxf(fabsf(qp[0]), fabsf(qp[1])), fmaxf(fabsf(qp[2]), fabsf(qp[3])))) >> 23;
      u32x4 qo;
#pragma unroll
      for (int i = 0; i < 4; ++i) qo[i] = ((unsigned)min((int)__builtin_rintf(__builtin_ldexpf(qp[i], 145 - (int)ex)), 524287) << 12) | (((ex >> (2 * i)) & 3u) << 10) | T[i];
      st128(qo, rws, lane < 16 ? WS_Q + (unsigned)qline * 1024u + qpart * 256u + lane * 16u : OOB);
    } else if (a == 2) {
      unsigned off[3];
      u32x4 v[3];
#pragma unroll
      for (int j = 0; j < 3; ++j) {
        const int ql = 3 * h + j;
        off[j] = lane < (ql >= 73 || (ql & 7) == 3 ? 48 : 32) ? WS_Q + (unsigned)ql * 1024u + lane * 16u : OOB;
        v[j] = T;
      }
      while (__builtin_amdgcn_s_memrealtime() < tk0 + QT) __builtin_amdgcn_s_sleep(4);
      SCHED();
      poll<3, QS, 0, 0>(rws, off, T, v);
      SCHED();
#pragma unroll
      for (int j = 0; j < 3; ++j) {
        const int ex = (int)(((v[j][0] >> 10) & 3u) | ((v[j][1] >> 8) & 12u) | ((v[j][2] >> 6) & 48u) | ((v[j][3] >> 4) & 192u)) - 145;
        f32x4 qf;
#pragma unroll
        for (int i = 0; i < 4; ++i) { float t = __builtin_ldexpf((float)((int)v[j][i] >> 12), ex); t = tr16(t, t); qf[i] = tr32(t, t); }
        if (lane < 16) *(u32x2*)&L.qs[j * 64 + lane * 4] = u32x2{pk(qf[0], qf[1]), pk(qf[2], qf[3])};
      }
      if (lane == 0) *(u32x4*)L.tag = T;
      lds_post(&L.cnt[1], lane);
    } else if (a == 3) {
      st128(T, rws, (s == (h & 7) && lane < 32) ? WS_PR + (unsigned)h * 256u + (lane & 15) * 16u + (lane >> 4) * TWIN : OOB);
      asm volatile("" ::"v"(pf[0]), "v"(pf[1]), "v"(pf[2]));
    }
    asm volatile(".Lcode_end2:");
    UNDEF8(wt);
  }

  const int wave0 = wave, tk1 = tk;
  {
    int lane = threadIdx.x & 63, wave = wave0, b = blockIdx.x, tk = tk1;
    asm volatile("" : "+v"(lane));
    asm volatile("" : "+s"(wave), "+s"(b), "+s"(tk));
    const int g = lane >> 3, c = lane & 7, h = b >> 3, s = b & 7, a = wave - NSW;
    const bool stream = wave < NSW;
    KARGS;
    const gptr w1 = ARG(5), w3 = ARG(6), w2 = ARG(7), out = ARG(8), ws = ARG(9);
    const rsrc_t rws = mk(ws, WS_END), rx = mk(ARG(0), D * 2);
    auto near = [&]() { lds_wait(&L.near, 1); return (unsigned)__builtin_amdgcn_readfirstlane(L.near) - 1u; };
    float fw = 0.f;
    if (a == 3) {
      const unsigned half = (__builtin_amdgcn_s_getreg(20 | (3 << 11)) >> 2) & 1u;
      while (__builtin_amdgcn_s_memrealtime() < tk0 + PT) __builtin_amdgcn_s_sleep(8);
      unsigned tt[2] = {~0u, ~0u};
#pragma unroll
      for (int r = 0; r < 4; ++r) {
        unsigned long long t0, t1;
        unsigned x;
        asm volatile("s_memtime %0\n\ts_waitcnt lgkmcnt(0)\n\ts_load_dword %2, %3, %4\n\ts_waitcnt lgkmcnt(0)\n\ts_memtime %1\n\ts_waitcnt lgkmcnt(0)"
                     : "=&s"(t0), "=&s"(t1), "=&s"(x) : "s"(ws + (WS_PR + h * 256) / 2), "n"((r >> 1) * 128 + (r & 1) * TWIN) : "memory");
        tt[r & 1] = min(tt[r & 1], (unsigned)(t1 - t0));
      }
      const int dt = max(-256, min(256, (int)(tt[0] - tt[1])));
      lds_wait(&L.cnt[0], NSW);
      const f32x2 mlw = *(const f32x2*)L.ml[c];
      const float mwg = asf(asu(max8(mlw[0])) & ~0x3FFu);
      const float lwg = sum8(mlw[1] * __builtin_amdgcn_exp2f((mlw[0] - mwg) * L2E));
      const u32x4 own = tagged(f32x4{mwg, lwg, (float)(half ? -dt : dt), 0.f}, T);
      st128(own, rws, (lane == 0 ? WS_ML + (unsigned)b * 128u : OOB));
      f32x4 mo[1];
      gather<0, MS>(rws, ((lane < 8 && lane != s) ? WS_ML + (unsigned)(h * 8 + lane) * 128u : OOB), T, mo);
      if (lane == s) mo[0] = asf4(own ^ T);
      const unsigned pol = sum8(mo[0][2]) > 0.f;
      if (lane == 0) __hip_atomic_store(&L.near, 1u + ((pol ^ half) & 1u) * TWIN, __ATOMIC_RELAXED, __HIP_MEMORY_SCOPE_WORKGROUP);
      const float mh = max8(mo[0][0]);
      fw = __builtin_amdgcn_exp2f((mlw[0] - mh) * L2E) * __builtin_amdgcn_rcpf(sum8(mo[0][1] * __builtin_amdgcn_exp2f((mo[0][0] - mh) * L2E)));
    }
    if (a == 3) {
      const int dq = min(lane, 47);
      unsigned src = LOFF(L.ored[0][4 * dq]);
      unsigned so = (lane < 48 ? WS_O + (unsigned)s * O_ROW + orow((unsigned)h * (DH * 4) + lane * 16u) : OOB);
      float f[NSW];
#pragma unroll
      for (int w = 0; w < NSW; ++w) { f[w] = rdl(fw, w); asm volatile("" : "+s"(f[w])); }
      asm volatile("" : "+v"(src), "+v"(so));
      f32x4 t[NSW];
      if (tk >= NF + NP) {
        bar();
#pragma unroll
        for (int w = 0; w < NSW; ++w) t[w] = *LAT(const f32x4, src + w * DH * 4);
      } else {
        await<2>(&L.flag[c], 64);
#pragma unroll
        for (int w = 0; w < NSW; ++w) t[w] = *LAT(const f32x4, src + w * DH * 4);
      }
#pragma unroll
      for (int w = 0; w < NSW; ++w) t[w] *= f[w];
      const f32x4 o = ((t[0] + t[1]) + (t[2] + t[3])) + ((t[4] + t[5]) + (t[6] + t[7]));
      const u32x4 ot = tagged(o, T);
      st128(ot, rws, so);
      st128(ot, rws, so + TWIN);
    }
    if (tk < NF) {
      if (wave < 10) {
        u32x4 wb[16];
        unsigned xv;
        {
          const rsrc_t rw2 = mk(w2, FF * D * 2);
          xv = __builtin_amdgcn_raw_buffer_load_b16(rx, (unsigned)(64 * wave + hrow(lane)) * 2u, 0, 0);
          if (!stream) {
            const gptr pw = ((lane & 8) ? w3 : w1) + (64 * wave + hrow(klane(lane))) * FF + 64 * tk + 8 * c;
#pragma unroll
            for (int t = 0; t < 16; ++t) {
              VMWAIT(DW - 1);
              wb[t] = __builtin_nontemporal_load((const __attribute__((address_space(1))) u32x4*)(pw + hrow(krow(t)) * FF));
              SCHED();
            }
            VMWAIT(16);
            lds_post(&L.cnt[3], lane, 256u);
            await<8>(&L.flag[c], 8);
#pragma unroll
            for (int t = 0; t < 8; ++t) {
              VMWAIT(WW - 1);
              wt[t] = ld128<AUX_NT>(rw2, (unsigned)(klane(lane) + (lane & 8)) * 1280u + c * 16u, (64 * tk + krow2(t)) * 1280 + wave * 128);
              SCHED();
            }
          }
          if (stream) {
            lds_wait(&L.cnt[3], 512);
#pragma unroll
            for (int t = 0; t < 16; ++t) wb[t] = L.wst[(lane >> 3) & 1][64 * wave + hrow(klane(lane)) + hrow(krow(t))][c];
          }
        }
        u32x8 b1[8], b2[4];
        turn(wb, b1); turn2(wt, b2);
        SCHED();
        T = *(const u32x4*)L.tag;
        int idx = smf_idx(lane & 8), idx2 = smf_idx(lane & 1);
        unsigned fdst = LOFF(L.fred[wave][(lane >> 5) * 64 + 2 * c + (g & 1)]);
        {
          const u32x4 ones = {0x3F803F80u, 0x3F803F80u, 0x3F803F80u, 0x3F803F80u};
#pragma unroll
          for (int i = 0; i < 4; ++i) *LAT(float, fdst + (LOFF(L.cred) - LOFF(L.fred)) + i * 64) = tr32(smf(ones, b1[i], idx), smf(ones, b1[i + 4], idx));
        }
        raise(&L.flag[8 + wave]);
        f32x4 pv[3];
        float xf = lo16(xv);
        if (lane & 16) fdst = LOFF(L.pred);
        unsigned hdst = (lane == 0 || lane == 40) ? LOFF(L.hsum[0][2 * wave + (g & 1)]) : LOFF(L.pred), asrc = LOFF(L.act[(lane & 48) + 4 * (lane & 2)]);
        unsigned as0 = lane & 8 ? LOFF(L.act[64]) : asrc, as1 = lane & 8 ? asrc : LOFF(L.act[64]);
        unsigned so = ((lane & 8) == 0 ? WS_P3 + prow(tk) + (lane & 16) * (TWIN / 16) + (unsigned)wave * 256u + (lane >> 5) * 128u + c * 16u : OOB);
        asm volatile("" : "+v"(fdst), "+v"(hdst), "+v"(xf), "+v"(as0), "+v"(as1), "+v"(so), "+v"(idx), "+v"(idx2));
        f32x4 d[8], e[2];
        zero(d);
        u32x4 ones = {0x3F803F80u, 0x3F803F80u, 0x3F803F80u, 0x3F803F80u};
        asm volatile("" : "+v"(ones));
        gather<32768, PS + 256 * PLS + 65536 * PK>(rws, WS_P2 + near() + prow(lane >> 4) + (unsigned)wave * 256u + (unsigned)(lane & 15) * 16u, T, pv);
        {
          const f32x4 ps = (pv[0] + pv[1]) + pv[2];
          const float at = tr16(tr32(ps[0], ps[2]), tr32(ps[1], ps[3]));
          const float hv = bfr(xf + bfr(at));
          const unsigned hp = phi(asu(hv), asu(dpp<XOR1>(hv)));
          const u32x4 a0 = {hp, shl<2>(hp), shl<4>(hp), shl<6>(hp)};
          const float s1 = mfma(a0, ones), s2 = mfma(a0, a0);
          SCHED();
#pragma unroll
          for (int i = 0; i < 2; ++i) d[i] = smf4(a0, b1[(i >> 1) + 4 * (i & 1)], d[i], idx);
          SCHED();
          *LAT(float, hdst) = s1; *LAT(float, hdst + 128) = s2;
          SCHED();
#pragma unroll
          for (int i = 2; i < 8; ++i) d[i] = smf4(a0, b1[(i >> 1) + 4 * (i & 1)], d[i], idx);
#pragma unroll
          for (int i = 0; i < 4; ++i) *LAT(float, fdst + i * 64) = tr32(d[2 * i][0], d[2 * i + 1][0]);
        }
        bar();
        zero(e);
        bar();
        const u32x4 a0 = *LAT(const u32x4, as0), a1 = *LAT(const u32x4, as1);
        f32x4 d0 = smf4(a0, b2[0], e[0], idx2), d1 = smf4(a0, b2[1], e[1], idx2);
        SCHED();
        d0 = smf4(a1, b2[2], d0, idx2); d1 = smf4(a1, b2[3], d1, idx2);
        st128(tagged(f32x4{d0[0] + dpp<ROR8>(d0[2]), d0[1] + dpp<ROR8>(d0[3]), d1[0] + dpp<ROR8>(d1[2]), d1[1] + dpp<ROR8>(d1[3])}, T), rws, so);
        LEAVE();
      }
    } else if (a == 3 && tk >= NF + NP && tk < NF + NP + NR) {
      const int j = tk - NF - NP, col0 = 64 * (j >> 3) + 16 * (j & 3) + (j & 4) + 8 * (lane & 1), e = 2 * (lane >> 5) + ((lane >> 4) & 1);
      const unsigned xv = __builtin_amdgcn_raw_buffer_load_b16(rx, (unsigned)(col0 + e) * 2u, 0, 0);
      const unsigned fo = near() + prow(lane >> 1);
      unsigned so = (lane & 14) == 0 ? (unsigned)(col0 + e) * 2u : OOB;
      auto fold = [&](f32x4 f) {
        const float r = tr16(tr32(f[0], f[2]), tr32(f[1], f[3]));
        return sum21(r + dpp<ROR8>(r));
      };
      f32x4 pv[1];
      {
        while (__builtin_amdgcn_s_memrealtime() < tk0 + G3) __builtin_amdgcn_s_sleep(2);
        __builtin_amdgcn_raw_buffer_store_b16((unsigned short)0, mk(out, D * 2), so, 0, AUX_SC1 | 1);
        const unsigned x = ld32<AUX_SC1>(rws, lane < 48 ? WS_P3 + fo + (unsigned)j * 32u : OOB, 0);
        asm volatile("" ::"v"(x));
      }
      while (__builtin_amdgcn_s_memrealtime() < tk0 + GR) __builtin_amdgcn_s_sleep(2);
      gather<0, HS>(rws, (lane < 24 ? WS_P2 + fo + (unsigned)col0 * 4u : OOB), T, pv);
      float hsel = bfr(lo16(xv) + bfr(fold(pv[0])));
      asm volatile("" : "+v"(hsel), "+v"(so));
      gather<0, RS>(rws, (lane < 48 ? WS_P3 + fo + (unsigned)j * 32u + (lane & 1) * 16u : OOB), T, pv);
      __builtin_amdgcn_raw_buffer_store_b16((unsigned short)(pk(hsel + bfr(fold(pv[0])), 0.f) & 0xFFFFu), mk(out, D * 2), so, 0, AUX_SC1 | 1);
      LEAVE();
    }
    if (a == 2) {
      if (tk < NF) {
        const int li = c * 16 + g * 2;
        if (lane < 4) *(unsigned*)&L.act[64 + 2 * lane] = 0u;
        await<2>(&L.flag[8 + (lane & 15)], 64);
        f32x2 cs = {0.f, 0.f};
#pragma unroll
        for (int j = 0; j < 10; ++j) cs += *(const f32x2*)&L.cred[j][li];
        unsigned src = LOFF(L.fred[0][li]), adst = LOFF(L.act[lane]);
        asm volatile("" : "+v"(cs), "+v"(src), "+v"(adst));
        unsigned* const hs = &L.hsum[0][lane];
        POLL_LOOP {
          if (__builtin_popcountll(__builtin_amdgcn_ballot_w64(__hip_atomic_load(hs, __ATOMIC_RELAXED, __HIP_MEMORY_SCOPE_WORKGROUP) != ~0u)) >= 28) break;
          __builtin_amdgcn_s_sleep(2);
        }
        unsigned sv, r0, r1, r2, r3, cap = SPIN_CAP;
#define LOOK(N, O) "ds_read_b32 %" #N ", %6\n\ts_waitcnt lgkmcnt(3)\n\tv_cmp_eq_u32 vcc, -1, %" #O "\n\tv_mov_b32 %0, %" #O "\n\ts_cbranch_vccz 2f\n\t"
        asm volatile("ds_read_b32 %1, %6\n\tds_read_b32 %2, %6\n\tds_read_b32 %3, %6\n"
                     "1:\n\t" LOOK(4, 1) LOOK(1, 2) LOOK(2, 3) LOOK(3, 4) "s_sub_u32 %5, %5, 1\n\ts_cbranch_scc0 1b\n"
                     "2:"
                     : "=&v"(sv), "=&v"(r0), "=&v"(r1), "=&v"(r2), "=&v"(r3), "+s"(cap) : "v"(LOFF(*hs) + (unsigned)(size_t)&L) : "vcc", "scc", "memory");
        SCHED();
        f32x2 t[10];
        asm volatile("s_barrier" ::: "memory");
        SCHED();
#pragma unroll
        for (int j = 0; j < 10; ++j) t[j] = *LAT(const f32x2, src + j * 512);
        SCHED();
        float s1 = sum8(asf(sv));
        s1 += dpp<ROR8>(s1);
        s1 = tr16(s1, s1);
        const auto s12 = __builtin_amdgcn_permlane32_swap(asu(s1), asu(s1), false, false);
        const float mean = asf(s12[0]) * (1.f / D), s2 = asf(s12[1]);
        float rden = rstd(bfr(fmaxf(s2 * (1.f / D) - mean * mean, 0.f)));
        cs *= -bfr(mean);
        asm volatile("" : "+v"(cs), "+v"(rden));
        SCHED();
        const f32x2 gu = ((((t[0] + t[1]) + (t[2] + t[3])) + ((t[4] + t[5]) + (t[6] + t[7]))) + ((t[8] + t[9]) + cs)) * rden;
        asm volatile("" ::"v"(r0), "v"(r1), "v"(r2), "v"(r3), "v"(gu));
        const unsigned gp = pk(gu[0], gu[1]);
        const float gt = lo16(gp);
        *LAT(unsigned short, adst) = (unsigned short)(pk(bfr(gt * __builtin_amdgcn_rcpf(1.f + __builtin_amdgcn_exp2f(-gt * L2E))) * hi16(gp), 0.f) & 0xFFFFu);
        bar();
        LEAVE();
      }
      if (tk >= NF && tk < NF + NP) {
        unsigned src = LOFF(L.pred[0][4 * (lane & 15)]);
        unsigned so = (lane < 32 ? WS_P2 + prow((tk - NF) / 10) + (lane & 16) * (TWIN / 16) + (unsigned)((tk - NF) % 10) * 256u + (lane & 15) * 16u : OOB);
        asm volatile("" : "+v"(src), "+v"(so));
        f32x4 t[NSW];
        bar();
#pragma unroll
        for (int w = 0; w < NSW; ++w) t[w] = *LAT(const f32x4, src + w * 256);
        st128(tagged(((t[0] + t[1]) + (t[2] + t[3])) + ((t[4] + t[5]) + (t[6] + t[7])), T), rws, so);
        LEAVE();
      }
    }
    if (!stream) {
      if (tk < NF) {
        if (a == 3) {
          bar();
          st64(e1, rws, tk == 0 && lane == 0 ? WS_EPOCH : OOB);
          bar();
        }
      }
      else if (tk < NF + NP ? a != 2 : a != 3) bar();
      LEAVE();
    }
  }
  asm volatile(".Lcode_end:");
}

}

extern "C" void kernel_launch(void* const* d_in, const int* in_sizes, int n_in, void* d_out, int out_size, void* d_ws, size_t ws_size,
                              hipStream_t stream) {
  Plan plan;
  for (int b = 0; b < NWG; ++b) {
    const int h = b >> 3, s = b & 7, e = (s - h - 1) & 7;
    const bool big = h >= NF, slow = !big && (h & 7) == 3;
    const int kp = big ? 512 : slow ? KP2 : KP1, kn = big ? 512 : slow ? KN2 : KN1, kf = WIN - 2 * kn - 5 * kp, nk = e == 7 ? kf : e < 2 ? kn : kp;
    int k0 = 0;
    for (int j = 0; j != e; ++j) k0 += j < 2 ? kn : kp;
    const int qi = big ? 96 + s * 8 + h - NF : e < 2 ? 2 * h + e : 46 + (slow ? 21 + (h >> 3) : h - ((h + 4) >> 3)) * 5 + e;
    const bool qjob = (big || e != 7) && qi < 160;
    const int q2 = qi - 96, q3 = qi < 32 ? qi : (qi - 32) >> 1;
    const int qline = q2 >= 0 ? q2 + q2 / 7 + (q2 % 7 >= 3) : q3 < 9 ? q3 * 8 + 3 : 64 + q3, qpart = q2 >= 0 ? !big : qi < 32 ? 2 : qi & 1;
    const int nl = big ? NB : q2 >= 0 ? 20 - NB : qpart == 2 ? 8 : 6, row0 = (q2 >= 0 ? 32 * NB : 192) * qpart;
    plan.w[3 * b] = (unsigned)(h * SK + (SK - WIN) + k0) * (DH * 2);
    plan.w[3 * b + 1] = qjob ? (unsigned)(row0 * 12288 + qline * 128) : OOB;
    plan.w[3 * b + 2] = (unsigned)(nk | (qjob ? nl << 10 | qpart << 14 : 0) | job_of(h, s) << 16);
  }
  plan.w[3 * NWG] = plan.w[3 * NWG + 1] = 0u;
  hipLaunchKernelGGL(k_fused, dim3(NWG), dim3(NTHR), 0, stream, (const unsigned short*)d_in[0], (const unsigned short*)d_in[1],
                     (const unsigned short*)d_in[2], (const unsigned short*)d_in[3], (const unsigned short*)d_in[4],
                     (const unsigned short*)d_in[6], (const unsigned short*)d_in[7], (const unsigned short*)d_in[8],
                     (unsigned short*)d_out, (unsigned char*)d_ws, plan);
}
```
